```python
import torch

def _softmax(x, axis=-1):
    return torch.softmax(x, dim=axis)
def _gelu(x):
    return torch.nn.functional.gelu(x, approximate="tanh")
def _silu(x):
    return torch.nn.functional.silu(x)
def _sigmoid(x):
    return torch.sigmoid(x)
def _layernorm(x, axis=-1, eps=1e-5):
    m = x.mean(dim=axis, keepdim=True)
    v = x.var(dim=axis, keepdim=True, unbiased=False)
    return (x - m) / torch.sqrt(v + eps)
def _scatter_add(tgt, idx, src, axis=0):
    return tgt.clone().index_add_(axis, idx, src)
def _segment_sum(src, seg, n):
    out = torch.zeros((n,) + src.shape[1:], dtype=src.dtype)
    return out.index_add_(0, seg, src)
def _pad(x, pw):
    flat = tuple(v for pair in reversed(pw) for v in pair)
    return torch.nn.functional.pad(x, flat)

kernel_name = 'concat_heads__ssm_conv_h3__linear_attn_block__prompt'

def setup_inputs():

    _w = (lambda a: (lambda t: t.to(torch.bfloat16) if t.is_floating_point() else t)(torch.as_tensor(a)))
    def _A_setup():
        B, S, D = 1, 2048, 640
        ks = 4
        return {
            "q": _w((lambda *s: torch.randn(*s, dtype=torch.bfloat16) * 0.05)(B, S, D)),
            "k": _w((lambda *s: torch.randn(*s, dtype=torch.bfloat16) * 0.4)(B, S, D)),
            "v": _w((lambda *s: torch.randn(*s, dtype=torch.bfloat16) * 0.4)(B, S, D)),
            "shift_k": _w((lambda *s: torch.randn(*s, dtype=torch.bfloat16) * 0.4)(D, ks) * 0.5),
            "ssm_k": _w((lambda *s: torch.randn(*s, dtype=torch.bfloat16) * 0.4)(D, S) * (1.0 / S ** 0.5)),
            "ssm_D": _w((lambda *s: torch.randn(*s, dtype=torch.bfloat16) * 0.4)(D)),
        }
    _A_in = _A_setup()

    def _B_setup():
        B, S, D = 1, 2048, 640
        return {
            "x": _w((lambda *s: torch.randn(*s, dtype=torch.bfloat16) * 0.05)(B, S, D)),
            "wqkv": _w((lambda *s: torch.randn(*s, dtype=torch.bfloat16) * 0.8)(D, 3 * D)),
            "wo": _w((lambda *s: torch.randn(*s, dtype=torch.bfloat16) * 0.05)(D, D)),
        }
    _B_in = _B_setup()

    return {
        "A_q": _A_in["q"],
        "A_k": _A_in["k"],
        "A_v": _A_in["v"],
        "A_shift_k": _A_in["shift_k"],
        "A_ssm_k": _A_in["ssm_k"],
        "A_ssm_D": _A_in["ssm_D"],
        "B_x": _B_in["x"],
        "B_wqkv": _B_in["wqkv"],
        "B_wo": _B_in["wo"],
    }

def reference(A_q, A_k, A_v, A_shift_k, A_ssm_k, A_ssm_D, B_x, B_wqkv, B_wo):
    def _A_ref(q, k, v, shift_k, ssm_k, ssm_D):
        B, S, D = q.shape
        ks = shift_k.shape[-1]
        k_pad = _pad(k, ((0, 0), (ks - 1, 0), (0, 0)))
        k_shift = torch.zeros_like(k)
        for t in range(ks):
            k_shift = k_shift + k_pad[:, t:t + S, :] * shift_k[:, ks - 1 - t][None, None, :]
        kv = k_shift * v
        L = 2 * S
        kv_pad = _pad(kv, ((0, 0), (0, S), (0, 0)))
        kf_pad = _pad(ssm_k, ((0, 0), (0, S)))
        kv_f = (lambda x, axes=(-2,-1): torch.fft.rfft2(x.float(), dim=axes))(kv_pad[:, None, :, :], axes=(1, 2))
        kf_f = (lambda x, axes=(-2,-1): torch.fft.rfft2(x.float(), dim=axes))(kf_pad[:, None, :], axes=(1, 2))
        kf_f = (lambda x, *d: x.permute(*d))(kf_f, 1, 2, 0)
        y_f = kv_f * kf_f[None, :, :, :]
        y = (lambda x, s, axes=(-2,-1): torch.fft.irfft2(x, s=s, dim=axes).to(torch.bfloat16))(y_f, (1, L), axes=(1, 2))[:, 0, :S, :]
        y = y + kv * ssm_D[None, None, :]
        return q * y

    def _B_ref(x, wqkv, wo):
        B, S, D = x.shape
        H = 32; Dh = D // H
        qkv = (lambda x, *d: x.permute(*d))((x @ wqkv).reshape(B, S, 3, H, Dh), 2, 0, 3, 1, 4)
        q, k, v = qkv[0], qkv[1], qkv[2]
        phi = lambda t: torch.where(t > 0, t + 1, torch.exp(t)) + 1e-6
        pq, pk = phi(q), phi(k)
        kv = torch.einsum("bhsd,bhse->bhde", pk, v)
        z = torch.einsum("bhsd->bhd", pk)
        num = torch.einsum("bhqd,bhde->bhqe", pq, kv)
        den = torch.einsum("bhqd,bhd->bhq", pq, z)[..., None] + 1e-6
        o = num / den
        return (lambda x, *d: x.permute(*d))(o, 0, 2, 1, 3).reshape(B, S, D) @ wo

    a = _A_ref(A_q, A_k, A_v, A_shift_k, A_ssm_k, A_ssm_D)
    b = _B_ref(B_x, B_wqkv, B_wo)
    return torch.concatenate([a, b], axis=-1)
```

```cpp
#include <hip/hip_runtime.h>
#include <hip/hip_bf16.h>
typedef unsigned short u16;
__device__ __forceinline__ float bf2f(u16 x){ return __uint_as_float(((unsigned)x)<<16); }
__device__ __forceinline__ u16 f2bf(float f){ __hip_bfloat16 b = __float2bfloat16(f); return *(u16*)&b; }
constexpr int S=2048, D=640, H=32, DH=20;

__global__ void k_u(const u16* k, const u16* v, const u16* sk, u16* uT){
  int idx = blockIdx.x*blockDim.x+threadIdx.x; if(idx>=S*D) return;
  int s = idx / D, c = idx % D;
  float acc=0;
  for(int j=3;j>=0;--j){ if(s-j>=0) acc += bf2f(k[(s-j)*D+c])*bf2f(sk[c*4+j]); }
  uT[c*S+s] = f2bf(acc*bf2f(v[idx]));
}
__global__ void k_conv(const u16* q, const u16* uT, const u16* h, const u16* Dv, u16* out){
  __shared__ float su[S], sh[S];
  int c = blockIdx.x;
  for(int i=threadIdx.x;i<S;i+=blockDim.x){ su[i]=bf2f(uT[c*S+i]); sh[i]=bf2f(h[c*S+i]); }
  __syncthreads();
  for(int s=threadIdx.x;s<S;s+=blockDim.x){
    float acc=0; for(int j=0;j<=s;++j) acc += sh[j]*su[s-j];
    float y = acc + su[s]*bf2f(Dv[c]);
    out[s*1280+c] = f2bf(bf2f(q[s*D+c])*y);
  }
}
__global__ void k_gemm(const u16* A, const u16* B, u16* C, int M,int N,int K,int ldc){
  int n = blockIdx.x*blockDim.x+threadIdx.x, m = blockIdx.y; if(n>=N) return;
  float acc=0; for(int k=0;k<K;++k) acc += bf2f(A[m*K+k])*bf2f(B[k*N+n]);
  C[m*ldc+n]=f2bf(acc);
}
__device__ __forceinline__ float phi(float t){ return (t>0? t+1.f : __expf(t)) + 1e-6f; }
__global__ void k_kv(const u16* qkv, float* kvz){
  int h = blockIdx.x; int t = threadIdx.x; if(t>=DH*21) return;
  int d = t/21, e = t%21; float acc=0;
  for(int s=0;s<S;++s){ float pk = bf2f(f2bf(phi(bf2f(qkv[s*1920+640+h*20+d])))); float vv = e<20? bf2f(qkv[s*1920+1280+h*20+e]) : 1.f; acc += pk*vv; }
  kvz[h*420+t]=bf2f(f2bf(acc));
}
__global__ void k_o(const u16* qkv, const float* kvz, u16* o){
  int idx = blockIdx.x*blockDim.x+threadIdx.x; if(idx>=S*D) return;
  int s = idx/D, c = idx%D, h=c/20, e=c%20; float num=0, den=0;
  for(int d=0;d<20;++d){ float pq = bf2f(f2bf(phi(bf2f(qkv[s*1920+h*20+d])))); num += pq*kvz[h*420+d*21+e]; den += pq*kvz[h*420+d*21+20]; }
  o[idx]=f2bf(num/(den+1e-6f));
}
extern "C" void kernel_launch(void* const* d_in, const int* in_sizes, int n_in, void* d_out, int out_size, void* d_ws, size_t ws_size, hipStream_t stream) {
  const u16 *q=(const u16*)d_in[0],*k=(const u16*)d_in[1],*v=(const u16*)d_in[2],*sk=(const u16*)d_in[3],*h=(const u16*)d_in[4],*Dv=(const u16*)d_in[5],*x=(const u16*)d_in[6],*wqkv=(const u16*)d_in[7],*wo=(const u16*)d_in[8];
  u16* out=(u16*)d_out; char* ws=(char*)d_ws;
  u16* uT=(u16*)ws; u16* qkv=(u16*)(ws+(4<<20)); float* kvz=(float*)(ws+(16<<20)); u16* o=(u16*)(ws+(20<<20));
  k_u<<<S*D/256,256,0,stream>>>(k,v,sk,uT);
  k_conv<<<D,256,0,stream>>>(q,uT,h,Dv,out);
  k_gemm<<<dim3(1920/128,S),128,0,stream>>>(x,wqkv,qkv,S,1920,640,1920);
  k_kv<<<H,448,0,stream>>>(qkv,kvz);
  k_o<<<S*D/256,256,0,stream>>>(qkv,kvz,o);
  k_gemm<<<dim3(640/128,S),128,0,stream>>>(o,wo,out+640,S,640,640,1280);
}
```

```cpp
#include <hip/hip_runtime.h>
typedef unsigned short u16;
typedef unsigned long long u64;
typedef unsigned v2u __attribute__((ext_vector_type(2)));
typedef unsigned v4u __attribute__((ext_vector_type(4)));
typedef int v4i __attribute__((ext_vector_type(4)));
typedef float v2f __attribute__((ext_vector_type(2)));
typedef float v4f __attribute__((ext_vector_type(4)));
typedef short v4s __attribute__((ext_vector_type(4)));
typedef __bf16 v2bf __attribute__((ext_vector_type(2)));
typedef __bf16 v8bf __attribute__((ext_vector_type(8)));
#define GAS __attribute__((address_space(1)))
#define LAS __attribute__((address_space(3)))
#define RLX_AGENT __ATOMIC_RELAXED, __HIP_MEMORY_SCOPE_AGENT
#define DEVINL __device__ __forceinline__

constexpr unsigned WS_FLAG = 4096;
constexpr unsigned FL_U = 0, FL_P = 128, FL_Y = 256, FL_O = 512;
constexpr unsigned WS_UT = 1u << 20, WS_YT = 4u << 20, WS_PART = 8u << 20, WS_OF = 9u << 20, WS_STAMP = 12u << 20;
constexpr unsigned WS_BYTES = 13u << 20;
constexpr unsigned PART_STRIDE = 3456;
constexpr unsigned OOB = 0x80000000u;
constexpr unsigned L_KVSLOT = 36864, L_QSLOT = 26624;
constexpr unsigned L_CS = 4160, L_UOFF = 4 * L_CS, L_CH = 21248;
constexpr unsigned L_KVCONV = 73728, L_QKIMG = 79872, L_QVIMG = 79872 + 22528, L_QCONV = 123904;
constexpr unsigned L_QX = 145408, L_KVC = 155648, L_DUMMY = 160768, L_CNT = 163328 + 256;
constexpr unsigned L_QT = 0, L_WO = 11264;

struct Params { const u16 *q, *k, *v, *sk, *h, *Dv, *x, *w, *wo; u16* out; char* ws; };

DEVINL v4i mk_rsrc(const void* p, unsigned bytes) {
  u64 a = (u64)p; v4i r; r.x = (int)(unsigned)a; r.y = (int)((unsigned)(a >> 32) & 0xffffu); r.z = (int)bytes; r.w = 0x00020000; return r;
}
DEVINL void dma(v4i rs, unsigned voff, unsigned soff, unsigned lds) {
  asm volatile("s_mov_b32 m0, %3\n\ts_nop 0\n\tbuffer_load_dwordx4 %0, %1, %2 offen lds" :: "v"(voff), "s"(rs), "s"(soff), "s"(lds) : "memory");
}
DEVINL void dma_sc1(v4i rs, unsigned voff, unsigned soff, unsigned lds) {
  asm volatile("s_mov_b32 m0, %3\n\ts_nop 0\n\tbuffer_load_dwordx4 %0, %1, %2 offen sc1 lds" :: "v"(voff), "s"(rs), "s"(soff), "s"(lds) : "memory");
}
template <int N> DEVINL void waitv() { asm volatile("s_waitcnt vmcnt(%0)" :: "n"(N) : "memory"); }
DEVINL void bar() { asm volatile("s_waitcnt lgkmcnt(0)\n\ts_barrier" ::: "memory"); }
#define SCHED() __builtin_amdgcn_sched_barrier(0)
DEVINL v4u lds128(unsigned a) { return *(const LAS v4u*)(size_t)a; }
DEVINL v2u lds64(unsigned a) { return *(const LAS v2u*)(size_t)a; }
DEVINL unsigned lds32(unsigned a) { return *(const LAS unsigned*)(size_t)a; }
DEVINL void sts128(unsigned a, v4u v) { *(LAS v4u*)(size_t)a = v; }
DEVINL void sts64(unsigned a, v2u v) { *(LAS v2u*)(size_t)a = v; }
DEVINL void sts32(unsigned a, unsigned v) { *(LAS unsigned*)(size_t)a = v; }
DEVINL v2u ldtr(unsigned a) { return __builtin_bit_cast(v2u, __builtin_amdgcn_ds_read_tr16_b64_v4i16((LAS v4s*)(size_t)a)); }
DEVINL v4u cat(v2u a, v2u b) { return (v4u){a.x, a.y, b.x, b.y}; }
DEVINL v4f mfma(v4u a, v4u b, v4f c) { return __builtin_amdgcn_mfma_f32_16x16x32_bf16(__builtin_bit_cast(v8bf, a), __builtin_bit_cast(v8bf, b), c, 0, 0, 0); }
DEVINL unsigned pk2(float a, float b) { v2f p = {a, b}; return __builtin_bit_cast(unsigned, __builtin_convertvector(p, v2bf)); }
DEVINL v2u pk4(v4f a) { return (v2u){pk2(a[0], a[1]), pk2(a[2], a[3])}; }
DEVINL float blo(unsigned u) { return __uint_as_float(u << 16); }
DEVINL float bhi(unsigned u) { return __uint_as_float(u & 0xffff0000u); }
DEVINL float phi(float t) { float e = __builtin_amdgcn_fmed3f(__builtin_amdgcn_exp2f(t * 1.44269504089f), 0.f, 1.f); return (fmaxf(t, 0.f) + 1e-6f) + e; }
DEVINL unsigned phi2(unsigned u) { return pk2(phi(blo(u)), phi(bhi(u))); }

DEVINL void set_flag(char* ws, unsigned idx, u64 tag) { __hip_atomic_store((GAS u64*)(ws + WS_FLAG + idx * 128u), tag, RLX_AGENT); }
DEVINL void wait_flags(char* ws, unsigned idx, u64 tag) {
  GAS u64* p = (GAS u64*)(ws + WS_FLAG + idx * 128u);
  for (unsigned spins = 0; spins < (1u << 16); ++spins) {
    u64 f = __hip_atomic_load(p, RLX_AGENT);
    if (__all(f == tag)) break;
    __builtin_amdgcn_s_sleep(1);
  }
  __builtin_amdgcn_fence(__ATOMIC_ACQUIRE, "wavefront");
  asm volatile("" ::: "memory");
}
DEVINL void arrive_flag(unsigned cnt, unsigned n, char* ws, unsigned idx, u64 tag, int lane) {
  waitv<0>();
  if (lane == 0) {
    unsigned old = __hip_atomic_fetch_add((LAS unsigned*)(size_t)cnt, 1u, __ATOMIC_RELAXED, __HIP_MEMORY_SCOPE_WORKGROUP);
    if (old == n - 1) set_flag(ws, idx, tag);
  }
}
#define ST(i)

template <int MT, bool SWAP>
DEVINL void gemm_compute(v4f (&acc)[MT][5], unsigned xa, unsigned wa, unsigned slot_bytes, bool do_uflag, char* ws, unsigned uidx, u64 tag, int tid) {
  v4u xp[MT], wp[5], xq[MT], wq[5];
  unsigned so = slot_bytes;
#define RD(X, W, hs) do { _Pragma("unroll") for (int n = 0; n < 5; ++n) W[n] = cat(ldtr(wa + so + (hs) * 5120 + n * 32), ldtr(wa + so + (hs) * 5120 + 2560 + n * 32)); \
    _Pragma("unroll") for (int m = 0; m < MT; ++m) X[m] = lds128(((xa + so) ^ ((hs) * 64)) + m * 2048); } while (0)
#define MM(X, W) do { _Pragma("unroll") for (int m = 0; m < MT; ++m) _Pragma("unroll") for (int n = 0; n < 5; ++n) acc[m][n] = SWAP ? mfma(W[n], X[m], acc[m][n]) : mfma(X[m], W[n], acc[m][n]); } while (0)
#define MIX() do { _Pragma("unroll") for (int i = 0; i < MT * 5; ++i) { __builtin_amdgcn_sched_group_barrier(0x100, MT == 4 ? 1 : 2, 0); __builtin_amdgcn_sched_group_barrier(0x8, 1, 0); } } while (0)
  bar(); SCHED();
  RD(xp, wp, 0);
  SCHED();
#pragma unroll 1
  for (int s = 0; s < 9; ++s) {
    RD(xq, wq, 1); MM(xp, wp); MIX(); SCHED();
    if (do_uflag && s == 3) waitv<0>();
    bar(); SCHED();
    if (do_uflag && s == 3 && tid == 0) set_flag(ws, uidx, tag);
    so = (so == 2 * slot_bytes) ? 0 : so + slot_bytes;
    RD(xp, wp, 0); MM(xq, wq); MIX(); SCHED();
  }
  RD(xq, wq, 1); MM(xp, wp); MIX(); SCHED();
  bar(); SCHED();
  MM(xq, wq);
#undef RD
#undef MM
#undef MIX
}

template <int MASK>
DEVINL void conv_wave(unsigned cb, unsigned dbits, char* ws, unsigned yoff, int lane) {
  constexpr int TMAX = (MASK & 128) ? 7 : (MASK & 64) ? 6 : (MASK & 32) ? 5 : 4;
  {
    v4u h0 = lds128(cb + 3 * L_CS + 32);
    v4u z = {0, 0, 0, 0};
    if (lane < 30) sts128(cb + L_UOFF + 4096 + lane * 16, z);
    else if (lane < 38) sts128(cb + ((lane - 30) >> 1) * L_CS + (lane & 1) * 16, z);
    else if (lane == 38) sts128(cb + 2 * L_CS + 32, (v4u){h0.x << 16, (h0.x >> 16) | (h0.y << 16), (h0.y >> 16) | (h0.z << 16), (h0.z >> 16) | (h0.w << 16)});
    else if (lane == 39) sts128(cb + 1 * L_CS + 32, (v4u){0, h0.x, h0.y, h0.z});
    else if (lane == 40) sts128(cb + 32, (v4u){0, h0.x << 16, (h0.x >> 16) | (h0.y << 16), (h0.y >> 16) | (h0.z << 16)});
    asm volatile("s_waitcnt lgkmcnt(0)" ::: "memory");
  }
  const int i = lane & 15, g = lane >> 4;
  unsigned ha = cb + (i & 3) * L_CS + 16 * g + 8 * (i >> 2) + 8;
  unsigned ua = cb + L_UOFF + 480 - 32 * i + 16 * g;
  v4f acc[8];
  v4u F[8], G[8], F2[8], G2[8];
#define CRD(F, G) do { _Pragma("unroll") for (int v = 0; v <= TMAX; ++v) { G[v] = lds128(ua + (7 - v) * 512); F[v] = cat(lds64(ha + v * 512), lds64(ha + v * 512 + 8)); } ha += 64; ua += 64; } while (0)
#define CMM(F, G) do { _Pragma("unroll") for (int w = 0; w <= TMAX; ++w) _Pragma("unroll") for (int t = w; t <= TMAX; ++t) if ((MASK >> t) & 1) acc[t] = mfma(F[w], G[t - w], acc[t]); } while (0)
#define CMIX() do { _Pragma("unroll") for (int j = 0; j < (MASK == 255 ? 36 : 9); ++j) { __builtin_amdgcn_sched_group_barrier(0x100, MASK == 255 ? 1 : 3, 0); __builtin_amdgcn_sched_group_barrier(0x8, 1, 0); } } while (0)
  CRD(F, G);
  {
    const int jt = 15 - i - 8 * g; v4u dg;
#pragma unroll
    for (int d = 0; d < 4; ++d) dg[d] = (jt == 2 * d) ? dbits : (jt == 2 * d + 1) ? (dbits << 16) : 0u;
#pragma unroll
    for (int t = 0; t <= TMAX; ++t) if ((MASK >> t) & 1) acc[t] = mfma(dg, G[t], (v4f){0, 0, 0, 0});
  }
  SCHED();
#pragma unroll 1
  for (int it = 0; it < 3; ++it) {
    CRD(F2, G2); CMM(F, G); CMIX(); SCHED();
    CRD(F, G); CMM(F2, G2); CMIX(); SCHED();
  }
  CRD(F2, G2); CMM(F, G); CMIX(); SCHED();
  CMM(F2, G2);
#undef CRD
#undef CMM
#undef CMIX
  const __amdgpu_buffer_rsrc_t rs = __builtin_amdgcn_make_buffer_rsrc(ws, 0, WS_BYTES, 0x00020000);
#pragma unroll
  for (int a = 0; a < 4; ++a) {
    if (!((MASK >> a) & 1)) continue;
    const int b = 7 - a;
    v2u X = pk4(acc[a]), Y = pk4(acc[b]);
    auto r0 = __builtin_amdgcn_permlane16_swap(X.x, Y.x, false, false);
    auto r1 = __builtin_amdgcn_permlane16_swap(X.y, Y.y, false, false);
    v4u o = {r0[0], r1[0], r0[1], r1[1]};
    __builtin_amdgcn_raw_buffer_store_b128(o, rs, yoff + ((g & 1) ? b : a) * 512 + 32 * i + 16 * (g >> 1), 0, 16);
  }
}

__global__ void __launch_bounds__(512, 2) fused_kernel(Params p) {
  __shared__ __attribute__((aligned(1024))) char smem[163840];
  const int tid = threadIdx.x, lane = tid & 63;
  const int wv = __builtin_amdgcn_readfirstlane(tid >> 6);
  const int b = blockIdx.x, xcd = b & 7, bi = b >> 3;
  const bool isQ = bi >= 16;
  const int rt = 4 * (xcd >> 1) + (bi & 3), g = 4 * (xcd & 1) + ((bi >> 2) & 3);
  asm volatile("" :: "v"(smem));
  ST(0);
  const u64 tag = (*(const u64*)p.ws) * 6364136223846793005ull + 1442695040888963407ull;
  const v4i rws = mk_rsrc(p.ws, WS_BYTES);
  const __amdgpu_buffer_rsrc_t bws = __builtin_amdgcn_make_buffer_rsrc(p.ws, 0, WS_BYTES, 0x00020000);
  const int l15 = lane & 15, grp = lane >> 4;
  const unsigned trl = (4 * grp + (l15 >> 2)) * 160 + 8 * (lane & 3);
  if (tid == 0) { sts32(L_CNT, 0); sts32(L_CNT + 4, 0); }
  const int lw = wv & 3;
  v2u pq[5];

  unsigned xo0 = 0, xo1 = 0, wo_[5] = {0, 0, 0, 0, 0};
  if (wv >= 4) {
    const unsigned r = lane >> 3;
    xo0 = (rt * 128 + lw * 32 + r) * 1280 + (((lane & 7) ^ (r >> 1)) << 4);
    xo1 = (xo0 ^ 64) + 8 * 1280;
#pragma unroll
    for (int j = 0; j < 5; ++j) { const unsigned ch = j * 64 + lane, rho = ch / 10, c = ch - rho * 10;
      wo_[j] = (8 * ((rho >> 2) & 3) + 4 * (rho >> 4) + (rho & 3)) * 3840 + c * 16; }
  }
  const v4i rx = mk_rsrc(p.x, 2048 * 1280), rw = mk_rsrc(p.w, 640 * 3840), rh = mk_rsrc(p.h, 640 * 4096);
  const int gr0 = (4 * xcd + (bi >> 3)) * 64, gct = bi & 7;

  auto load_h = [&](int c, int rho, unsigned cb) {
#pragma unroll
    for (int j = 0; j < 4; ++j) dma(rh, (j == 0 && lane == 0 && rho < 3) ? OOB : (unsigned)(lane * 16 + j * 1024 + 2 * rho + c * 4096 - 6), 0, cb + rho * L_CS + 32 + j * 1024);
  };
  auto load_u = [&](int c, unsigned cb) {
#pragma unroll
    for (int j = 0; j < 4; ++j) dma_sc1(rws, lane * 16, WS_UT + c * 4096 + j * 1024, cb + L_UOFF + j * 1024);
  };
  auto load_qwo = [&](int i0, int i1) {
    const v4i rq = mk_rsrc(p.q, 2048 * 1280), ro = mk_rsrc(p.wo, 640 * 1280);
#pragma unroll 1
    for (int i = i0 + lw; i < i1; i += 4) {
      if (i < 11) { const unsigned ch = i * 64 + lane, gq = ch / 81, e = ch - gq * 81, r = e / 10, c = e - r * 10;
        dma(rq, (e < 80 && gq < 8) ? (gr0 + gq * 8 + r) * 1280 + gct * 160 + c * 16 : OOB, 0, L_QT + i * 1024); }
      else { const unsigned ch = (i - 11) * 64 + lane, r = ch / 10, c = ch - r * 10;
        dma(ro, r * 1280 + gct * 160 + c * 16, 0, i * 1024); }
    }
  };

  if (!isQ) {
    const int c0 = 80 * g + 4 * rt;
    if (wv < 4) {
      v4f acc[4][5];
#pragma unroll
      for (int m = 0; m < 4; ++m)
#pragma unroll
        for (int n = 0; n < 5; ++n) acc[m][n] = (v4f){0, 0, 0, 0};
      const unsigned xa = ((wv >> 1) * 64 + l15) * 128 + ((grp ^ (l15 >> 1)) << 4);
      gemm_compute<4, false>(acc, xa, 16384 + (wv & 1) * 10240 + trl, L_KVSLOT, false, p.ws, 0, tag, tid);
      ST(1);
#pragma unroll
      for (int n = 0; n < 5; ++n)
#pragma unroll
        for (int t = 0; t < 2; ++t) sts128((((wv & 1) * 5 + n) * 4 + (wv >> 1) * 2 + t) * 1024 + lane * 16, cat(pk4(acc[2 * t][n]), pk4(acc[2 * t + 1][n])));
    } else {
      const unsigned wcol = (640 + 640 * (lw & 1) + 80 * g) * 2 + (lw >> 1) * 32 * 3840;
      const unsigned wl = 16384 + (lw & 1) * 10240 + (lw >> 1) * 5120;
      auto issue = [&](int s, unsigned so) {
#pragma unroll
        for (int j = 0; j < 5; ++j) dma(rw, wo_[j], wcol + s * 64 * 3840, so + wl + j * 1024);
#pragma unroll
        for (int j = 0; j < 4; ++j) dma(rx, (j & 1) ? xo1 : xo0, s * 128 + (j >> 1) * 16 * 1280, so + lw * 4096 + j * 1024);
      };
      issue(0, L_KVSLOT); issue(1, 2 * L_KVSLOT); issue(2, 0);
      waitv<18>(); bar();
      unsigned so = L_KVSLOT;
#pragma unroll 1
      for (int s = 1; s < 8; ++s) { waitv<9>(); bar(); issue(s + 2, so); so = (so == 2 * L_KVSLOT) ? 0 : so + L_KVSLOT; }
      waitv<9>(); bar();
#pragma unroll 1
      for (int rho = 0; rho < 4; ++rho) load_h(c0 + lw, rho, L_KVCONV + lw * L_CH);
      waitv<16>(); bar();
      bar();
    }
    bar();
    if (wv < 5) {
      const int a = wv;
      v4f d0 = {0, 0, 0, 0}, d1 = d0, d2 = d0, dz = d0;
      const v4u ones = {0x3f803f80u, 0x3f803f80u, 0x3f803f80u, 0x3f803f80u};
      const unsigned ka = a * 4096 + lane * 16;
      const unsigned vm = (5 + (a > 0 ? a - 1 : 0)) * 4096 + lane * 16, vc = (5 + a) * 4096 + lane * 16, vp = (5 + (a < 4 ? a + 1 : 4)) * 4096 + lane * 16;
#pragma unroll
      for (int t = 0; t < 4; ++t) {
        v4u kf = lds128(ka + t * 1024);
        v4u pk = {phi2(kf.x), phi2(kf.y), phi2(kf.z), phi2(kf.w)};
        d0 = mfma(pk, lds128(vm + t * 1024), d0); d1 = mfma(pk, lds128(vc + t * 1024), d1); d2 = mfma(pk, lds128(vp + t * 1024), d2); dz = mfma(pk, ones, dz);
      }
      const int Dq = 4 * a + grp, hh = Dq / 5;
      const unsigned pb = WS_PART + (g * 16 + rt) * PART_STRIDE + Dq * 168;
      const int e0 = 16 * (a - 1) + l15 - 20 * hh;
      __builtin_amdgcn_raw_buffer_store_b64(pk4(d0), bws, ((unsigned)e0 < 20u) ? pb + e0 * 8 : OOB, 0, 16);
      __builtin_amdgcn_raw_buffer_store_b64(pk4(d1), bws, ((unsigned)(e0 + 16) < 20u) ? pb + (e0 + 16) * 8 : OOB, 0, 16);
      __builtin_amdgcn_raw_buffer_store_b64(pk4(d2), bws, ((unsigned)(e0 + 32) < 20u) ? pb + (e0 + 32) * 8 : OOB, 0, 16);
      __builtin_amdgcn_raw_buffer_store_b64(pk4(dz), bws, (l15 == 0) ? pb + 160 : OOB, 0, 16);
      ST(2);
      arrive_flag(L_CNT, 5, p.ws, FL_P + g * 16 + rt, tag, lane);
      ST(3);
    } else if (wv == 7) {
      wait_flags(p.ws, FL_U + g * 16 + l15, tag);
#pragma unroll 1
      for (int c = 0; c < 4; ++c) load_u(c0 + c, L_KVCONV + c * L_CH);
    }
    waitv<0>(); bar();
    ST(4);
    if (wv < 4) {
      conv_wave<255>(L_KVCONV + wv * L_CH, p.Dv[c0 + wv], p.ws, WS_YT + (c0 + wv) * 4096, lane);
      ST(5);
      waitv<0>();
    } else load_qwo(0, 72);
    bar();
    if (tid == 0) set_flag(p.ws, FL_Y + g * 32 + rt, tag);
    if (wv >= 4) { load_qwo(72, 111); waitv<0>(); }
    bar();
  } else {
    const int iq = bi - 16, tt = 2 * xcd + (iq >> 3), cg = iq & 7, t0 = tt * 128;
    const int c0 = 80 * g + 64 + rt;
    {
      const v4i rk = mk_rsrc(p.k, 2048 * 1280), rv = mk_rsrc(p.v, 2048 * 1280);
#pragma unroll 1
      for (int i = wv; i < 43; i += 8) {
        const bool isv = i >= 22;
        const unsigned ch = (isv ? i - 22 : i) * 64 + lane, gq = ch / 81, e = ch - gq * 81, r = e / 10, c = e - r * 10;
        const int row = t0 + (int)(gq * 8 + r) - (isv ? 0 : 8);
        const bool ok = e < 80 && row >= t0 - 3 && row >= 0 && row < t0 + 128;
        const unsigned vo = ok ? row * 1280 + cg * 160 + c * 16 : OOB;
        if (isv) dma(rv, vo, 0, L_QKIMG + i * 1024); else dma(rk, vo, 0, L_QKIMG + i * 1024);
      }
    }
    if (wv < 4) {
      waitv<0>(); bar();
      ST(1);
#pragma unroll 1
      for (int wu = wv; wu < 10; wu += 4) {
        const int tg = 8 * (wu & 1) + (lane & 7), cp = 8 * (wu >> 1) + (lane >> 3), c = 80 * cg + 2 * cp;
        const v4u sw = *(const v4u*)(p.sk + c * 4);
        const unsigned ka = L_QKIMG + tg * 1296 + cp * 4, va = L_QVIMG + tg * 1296 + cp * 4;
        float k0[11], k1[11];
#pragma unroll
        for (int j = 0; j < 11; ++j) { const unsigned u = lds32(ka + (j < 3 ? (5 + j) * 160 : 1296 + (j - 3) * 160)); k0[j] = blo(u); k1[j] = bhi(u); }
        float u0[8], u1[8];
#pragma unroll
        for (int j = 0; j < 8; ++j) { const unsigned u = lds32(va + j * 160);
          u0[j] = (k0[j] * bhi(sw.y) + k0[j + 1] * blo(sw.y) + k0[j + 2] * bhi(sw.x) + k0[j + 3] * blo(sw.x)) * blo(u);
          u1[j] = (k1[j] * bhi(sw.w) + k1[j + 1] * blo(sw.w) + k1[j + 2] * bhi(sw.z) + k1[j + 3] * blo(sw.z)) * bhi(u); }
        const unsigned uo = WS_UT + c * 4096 + 2 * (2040 - t0 - 8 * tg);
        __builtin_amdgcn_raw_buffer_store_b128((v4u){pk2(u0[7], u0[6]), pk2(u0[5], u0[4]), pk2(u0[3], u0[2]), pk2(u0[1], u0[0])}, bws, uo, 0, 16);
        __builtin_amdgcn_raw_buffer_store_b128((v4u){pk2(u1[7], u1[6]), pk2(u1[5], u1[4]), pk2(u1[3], u1[2]), pk2(u1[1], u1[0])}, bws, uo + 4096, 0, 16);
      }
      ST(2);
      v4f acc[2][5];
#pragma unroll
      for (int m = 0; m < 2; ++m)
#pragma unroll
        for (int n = 0; n < 5; ++n) acc[m][n] = (v4f){0, 0, 0, 0};
      const unsigned xa = (wv * 32 + l15) * 128 + ((grp ^ (l15 >> 1)) << 4);
      gemm_compute<2, true>(acc, xa, 16384 + trl, L_QSLOT, true, p.ws, FL_U + cg * 16 + tt, tag, tid);
      ST(3);
#pragma unroll
      for (int n = 0; n < 5; ++n) { pq[n] = pk4(acc[0][n]); sts64(L_QX + wv * 2560 + n * 512 + lane * 8, pk4(acc[1][n])); }
      bar();
    } else {
      const unsigned wcol = 160 * g + (lw >> 1) * 32 * 3840;
      const unsigned wl = 16384 + (lw >> 1) * 5120;
      const unsigned w0 = (lw & 1) ? wo_[1] : wo_[0], w1 = (lw & 1) ? wo_[3] : wo_[2], w2 = (lw & 1) ? OOB : wo_[4];
      auto issue = [&](int s, unsigned so) {
        dma(rw, w0, wcol + s * 64 * 3840, so + wl + (lw & 1) * 1024);
        dma(rw, w1, wcol + s * 64 * 3840, so + wl + (lw & 1) * 1024 + 2048);
        dma(rw, w2, wcol + s * 64 * 3840, (lw & 1) ? L_DUMMY : so + wl + 4096);
#pragma unroll
        for (int j = 0; j < 4; ++j) dma(rx, (j & 1) ? xo1 : xo0, s * 128 + (j >> 1) * 16 * 1280, so + lw * 4096 + j * 1024);
      };
      issue(0, L_QSLOT); issue(1, 2 * L_QSLOT); issue(2, 0);
      waitv<21>(); bar();
      waitv<14>(); bar();
      unsigned so = L_QSLOT;
#pragma unroll 1
      for (int s = 1; s < 8; ++s) { waitv<7>(); bar(); issue(s + 2, so); so = (so == 2 * L_QSLOT) ? 0 : so + L_QSLOT; }
      waitv<7>(); bar();
      load_h(c0, lw, L_QCONV);
      waitv<4>(); bar();
      bar();
      if (wv == 7) { wait_flags(p.ws, FL_U + g * 16 + l15, tag); load_u(c0, L_QCONV); }
      waitv<0>();
      bar();
#pragma unroll
      for (int n = 0; n < 5; ++n) pq[n] = lds64(L_QX + lw * 2560 + n * 512 + lane * 8);
      load_qwo(0, 111);
    }
#pragma unroll
    for (int n = 0; n < 5; ++n) pq[n] = (v2u){phi2(pq[n].x), phi2(pq[n].y)};
    ST(4);
    if (wv < 4) {
      const unsigned db = p.Dv[c0], yo = WS_YT + c0 * 4096;
      if (wv == 0) conv_wave<0x81>(L_QCONV, db, p.ws, yo, lane);
      else if (wv == 1) conv_wave<0x42>(L_QCONV, db, p.ws, yo, lane);
      else if (wv == 2) conv_wave<0x24>(L_QCONV, db, p.ws, yo, lane);
      else conv_wave<0x18>(L_QCONV, db, p.ws, yo, lane);
      ST(5);
      arrive_flag(L_CNT, 4, p.ws, FL_Y + g * 32 + 16 + rt, tag, lane);
    } else if (wv == 7) wait_flags(p.ws, FL_P + g * 16 + l15, tag);
    bar();
    ST(6);
    if (wv < 7) {
      const unsigned po = (tid < 420) ? WS_PART + g * 16 * PART_STRIDE + tid * 8 : OOB;
      v2u r[16];
#pragma unroll
      for (int j = 0; j < 16; ++j) r[j] = __builtin_amdgcn_raw_buffer_load_b64(bws, po, j * PART_STRIDE, 16);
      v4f s = {0, 0, 0, 0};
#pragma unroll
      for (int j = 0; j < 16; ++j) { s[0] += blo(r[j].x); s[1] += bhi(r[j].x); s[2] += blo(r[j].y); s[3] += bhi(r[j].y); }
      sts64(L_KVC + tid * 8, pk4(s));
    }
    waitv<0>(); bar();
    ST(7);
    {
      const v2u z2 = {0, 0};
      auto kvf = [&](int a, int bb) { const int Dq = 4 * a + grp, e = 16 * bb + l15 - 20 * (Dq / 5); return lds64(L_KVC + (((unsigned)e < 20u) ? Dq * 21 + e : 420) * 8); };
      auto zf = [&](int a) { const int Dq = 4 * a + grp; return lds64(L_KVC + ((Dq / 5 == (l15 & 3)) ? Dq * 21 + 20 : 420) * 8); };
      const v4u s0 = cat(pq[0], pq[1]), s1 = cat(pq[2], pq[3]), s2 = cat(pq[4], z2);
      const v4f zero = {0, 0, 0, 0};
      v4f den = mfma(cat(zf(0), zf(1)), s0, zero); den = mfma(cat(zf(2), zf(3)), s1, den); den = mfma(cat(zf(4), z2), s2, den);
      v4f n0 = mfma(cat(kvf(0, 0), kvf(1, 0)), s0, zero);
      v4f n1 = mfma(cat(kvf(0, 1), kvf(1, 1)), s0, zero); n1 = mfma(cat(kvf(2, 1), z2), s1, n1);
      v4f n2 = mfma(cat(z2, kvf(1, 2)), s0, zero); n2 = mfma(cat(kvf(2, 2), kvf(3, 2)), s1, n2);
      v4f n3 = mfma(cat(kvf(2, 3), kvf(3, 3)), s1, zero); n3 = mfma(cat(kvf(4, 3), z2), s2, n3);
      v4f n4 = mfma(cat(z2, kvf(3, 4)), s1, zero); n4 = mfma(cat(kvf(4, 4), z2), s2, n4);
      const float r0 = __builtin_amdgcn_rcpf(den[0] + 1e-6f), r1 = __builtin_amdgcn_rcpf(den[1] + 1e-6f), r2 = __builtin_amdgcn_rcpf(den[2] + 1e-6f), r3 = __builtin_amdgcn_rcpf(den[3] + 1e-6f);
      n0 *= r0; n1 *= (grp < 1) ? r0 : r1; n2 *= (grp < 2) ? r1 : r2; n3 *= (grp < 3) ? r2 : r3; n4 *= r3;
      const int mt = rt * 8 + 2 * lw + (wv >> 2);
      const unsigned oo = WS_OF + (mt * 8 + g) * 2560;
      __builtin_amdgcn_raw_buffer_store_b128(cat(pk4(n0), pk4(n1)), bws, oo + lane * 16, 0, 16);
      __builtin_amdgcn_raw_buffer_store_b128(cat(pk4(n2), pk4(n3)), bws, oo + 1024 + lane * 16, 0, 16);
      __builtin_amdgcn_raw_buffer_store_b64(pk4(n4), bws, oo + 2048 + lane * 8, 0, 16);
      ST(8);
      waitv<0>();
      if (lane == 0) set_flag(p.ws, FL_O + mt * 8 + g, tag);
    }
  }
  ST(9);
  const __amdgpu_buffer_rsrc_t bout = __builtin_amdgcn_make_buffer_rsrc(p.out, 0, 2048 * 2560, 0x00020000);
  if (wv < 4) {
    const int mt = (gr0 >> 4) + wv;
    wait_flags(p.ws, FL_O + mt * 8 + (lane & 7), tag);
    ST(10);
    v4u of[20];
#pragma unroll
    for (int y = 0; y < 4; ++y) {
      const unsigned oo = WS_OF + (mt * 8 + 2 * y) * 2560;
      of[5 * y + 0] = __builtin_amdgcn_raw_buffer_load_b128(bws, lane * 16, oo, 16);
      of[5 * y + 1] = __builtin_amdgcn_raw_buffer_load_b128(bws, lane * 16 + 1024, oo, 16);
      of[5 * y + 2] = __builtin_amdgcn_raw_buffer_load_b128(bws, lane * 16 + 2560, oo, 16);
      of[5 * y + 3] = __builtin_amdgcn_raw_buffer_load_b128(bws, lane * 16 + 3584, oo, 16);
      of[5 * y + 4] = cat(__builtin_amdgcn_raw_buffer_load_b64(bws, lane * 8 + 2048, oo, 16), __builtin_amdgcn_raw_buffer_load_b64(bws, lane * 8 + 2048, oo + 2560, 16));
    }
    v4f acc[5];
#pragma unroll
    for (int n = 0; n < 5; ++n) acc[n] = (v4f){0, 0, 0, 0};
#pragma unroll
    for (int y = 0; y < 4; ++y) {
      const unsigned wb = L_WO + trl + y * 25600;
#pragma unroll
      for (int s = 0; s < 5; ++s) {
        const unsigned ra = (s == 4) ? 64 : (s >> 1) * 80 + (s & 1) * 32, rb = (s == 4) ? 144 : ra + 16;
#pragma unroll
        for (int n = 0; n < 5; ++n) acc[n] = mfma(cat(ldtr(wb + ra * 160 + n * 32), ldtr(wb + rb * 160 + n * 32)), of[5 * y + s], acc[n]);
      }
    }
    const unsigned ob = (gr0 + wv * 16 + l15) * 2560 + 1280 + gct * 160 + grp * 8;
#pragma unroll
    for (int n = 0; n < 5; ++n) __builtin_amdgcn_raw_buffer_store_b64(pk4(acc[n]), bout, ob + n * 32, 0, 0);
    ST(11);
    if (b == 0 && wv == 0) {
      wait_flags(p.ws, FL_U + lane, tag); wait_flags(p.ws, FL_U + 64 + lane, tag);
      if (lane == 0) __hip_atomic_store((GAS u64*)p.ws, tag, RLX_AGENT);
    }
  } else {
    wait_flags(p.ws, FL_Y + gct * 32 + (lane & 31), tag);
    ST(10);
#pragma unroll 1
    for (int cb = lw; cb < 5; cb += 4) {
      const int tg = lane & 7, cp = 8 * cb + (lane >> 3);
      const unsigned yo = WS_YT + (80 * gct + 2 * cp) * 4096 + 2 * (gr0 + 8 * tg);
      const v4u y0 = __builtin_amdgcn_raw_buffer_load_b128(bws, yo, 0, 16), y1 = __builtin_amdgcn_raw_buffer_load_b128(bws, yo, 4096, 16);
      const unsigned qa = L_QT + tg * 1296 + cp * 4;
#pragma unroll
      for (int j = 0; j < 8; ++j) { const unsigned u = lds32(qa + j * 160);
        const float a0 = (j & 1) ? bhi(y0[j >> 1]) : blo(y0[j >> 1]), a1 = (j & 1) ? bhi(y1[j >> 1]) : blo(y1[j >> 1]);
        sts32(qa + j * 160, pk2(blo(u) * a0, bhi(u) * a1)); }
      asm volatile("s_waitcnt lgkmcnt(0)" ::: "memory");
#pragma unroll
      for (int j = 0; j < 2; ++j) { const int row = j * 32 + (lane >> 1);
        const v4u o = lds128(L_QT + (row >> 3) * 1296 + (row & 7) * 160 + cb * 32 + (lane & 1) * 16);
        __builtin_amdgcn_raw_buffer_store_b128(o, bout, (gr0 + row) * 2560 + gct * 160 + cb * 32 + (lane & 1) * 16, 0, 0); }
    }
    ST(11);
  }
}

extern "C" void kernel_launch(void* const* d_in, const int* in_sizes, int n_in, void* d_out, int out_size, void* d_ws, size_t ws_size, hipStream_t stream) {
  Params p;
  p.q = (const u16*)d_in[0]; p.k = (const u16*)d_in[1]; p.v = (const u16*)d_in[2]; p.sk = (const u16*)d_in[3]; p.h = (const u16*)d_in[4];
  p.Dv = (const u16*)d_in[5]; p.x = (const u16*)d_in[6]; p.w = (const u16*)d_in[7]; p.wo = (const u16*)d_in[8];
  p.out = (u16*)d_out; p.ws = (char*)d_ws;
  fused_kernel<<<256, 512, 0, stream>>>(p);
}
```

```cpp
#include <hip/hip_runtime.h>
typedef unsigned short u16;
typedef unsigned long long u64;
typedef unsigned v2u __attribute__((ext_vector_type(2)));
typedef unsigned v4u __attribute__((ext_vector_type(4)));
typedef int v4i __attribute__((ext_vector_type(4)));
typedef float v2f __attribute__((ext_vector_type(2)));
typedef float v4f __attribute__((ext_vector_type(4)));
typedef short v4s __attribute__((ext_vector_type(4)));
typedef __bf16 v2bf __attribute__((ext_vector_type(2)));
typedef __bf16 v8bf __attribute__((ext_vector_type(8)));
#define GAS __attribute__((address_space(1)))
#define LAS __attribute__((address_space(3)))
#define RLX_AGENT __ATOMIC_RELAXED, __HIP_MEMORY_SCOPE_AGENT
#define DEVINL __device__ __forceinline__

constexpr unsigned WS_FLAG = 4096;
constexpr unsigned FL_U = 0, FL_P = 128, FL_Y = 256, FL_O = 512;
constexpr unsigned WS_UT = 1u << 20, WS_YT = 4u << 20, WS_PART = 8u << 20, WS_OF = 9u << 20, WS_STAMP = 12u << 20;
constexpr unsigned WS_BYTES = 13u << 20;
constexpr unsigned PART_STRIDE = 3456;
constexpr unsigned OOB = 0x80000000u;
constexpr unsigned L_KVSLOT = 36864, L_QSLOT = 26624;
constexpr unsigned L_CS = 4160, L_UOFF = 4 * L_CS, L_CH = 21248;
constexpr unsigned L_KVCONV = 73728, L_QKIMG = 79872, L_QVIMG = 79872 + 22528, L_QCONV = 123904;
constexpr unsigned L_QX = 145408, L_KVC = 155648, L_DUMMY = 160768, L_CNT = 163328 + 256;
constexpr unsigned L_QT = 0, L_WO = 11264;

struct Params { const u16 *q, *k, *v, *sk, *h, *Dv, *x, *w, *wo; u16* out; char* ws; };

DEVINL v4i mk_rsrc(const void* p, unsigned bytes) {
  u64 a = (u64)p; v4i r; r.x = (int)(unsigned)a; r.y = (int)((unsigned)(a >> 32) & 0xffffu); r.z = (int)bytes; r.w = 0x00020000; return r;
}
DEVINL void dma(v4i rs, unsigned voff, unsigned soff, unsigned lds) {
  asm volatile("s_mov_b32 m0, %3\n\ts_nop 0\n\tbuffer_load_dwordx4 %0, %1, %2 offen lds" :: "v"(voff), "s"(rs), "s"(soff), "s"(lds) : "memory");
}
DEVINL void dma_sc1(v4i rs, unsigned voff, unsigned soff, unsigned lds) {
  asm volatile("s_mov_b32 m0, %3\n\ts_nop 0\n\tbuffer_load_dwordx4 %0, %1, %2 offen sc1 lds" :: "v"(voff), "s"(rs), "s"(soff), "s"(lds) : "memory");
}
template <int N> DEVINL void waitv() { asm volatile("s_waitcnt vmcnt(%0)" :: "n"(N) : "memory"); }
DEVINL void bar() { asm volatile("s_waitcnt lgkmcnt(0)\n\ts_barrier" ::: "memory"); }
#define SCHED() __builtin_amdgcn_sched_barrier(0)
DEVINL v4u lds128(unsigned a) { return *(const LAS v4u*)(size_t)a; }
DEVINL v2u lds64(unsigned a) { return *(const LAS v2u*)(size_t)a; }
DEVINL unsigned lds32(unsigned a) { return *(const LAS unsigned*)(size_t)a; }
DEVINL void sts128(unsigned a, v4u v) { *(LAS v4u*)(size_t)a = v; }
DEVINL void sts64(unsigned a, v2u v) { *(LAS v2u*)(size_t)a = v; }
DEVINL void sts32(unsigned a, unsigned v) { *(LAS unsigned*)(size_t)a = v; }
DEVINL v2u ldtr(unsigned a) { return __builtin_bit_cast(v2u, __builtin_amdgcn_ds_read_tr16_b64_v4i16((LAS v4s*)(size_t)a)); }
DEVINL v4u cat(v2u a, v2u b) { return (v4u){a.x, a.y, b.x, b.y}; }
DEVINL v4f mfma(v4u a, v4u b, v4f c) { return __builtin_amdgcn_mfma_f32_16x16x32_bf16(__builtin_bit_cast(v8bf, a), __builtin_bit_cast(v8bf, b), c, 0, 0, 0); }
DEVINL unsigned pk2(float a, float b) { v2f p = {a, b}; return __builtin_bit_cast(unsigned, __builtin_convertvector(p, v2bf)); }
DEVINL v2u pk4(v4f a) { return (v2u){pk2(a[0], a[1]), pk2(a[2], a[3])}; }
DEVINL float blo(unsigned u) { return __uint_as_float(u << 16); }
DEVINL float bhi(unsigned u) { return __uint_as_float(u & 0xffff0000u); }
DEVINL float phi(float t) { float e = __builtin_amdgcn_fmed3f(__builtin_amdgcn_exp2f(t * 1.44269504089f), 0.f, 1.f); return (fmaxf(t, 0.f) + 1e-6f) + e; }
DEVINL unsigned phi2(unsigned u) { return pk2(phi(blo(u)), phi(bhi(u))); }

DEVINL void set_flag(char* ws, unsigned idx, u64 tag) { __hip_atomic_store((GAS u64*)(ws + WS_FLAG + idx * 128u), tag, RLX_AGENT); }
DEVINL void wait_flags(char* ws, unsigned idx, u64 tag) {
  GAS u64* p = (GAS u64*)(ws + WS_FLAG + idx * 128u);
  for (unsigned spins = 0; spins < (1u << 16); ++spins) {
    u64 f = __hip_atomic_load(p, RLX_AGENT);
    if (__all(f == tag)) break;
    __builtin_amdgcn_s_sleep(1);
  }
  __builtin_amdgcn_fence(__ATOMIC_ACQUIRE, "wavefront");
  asm volatile("" ::: "memory");
}
DEVINL void arrive_flag(unsigned cnt, unsigned n, char* ws, unsigned idx, u64 tag, int lane) {
  waitv<0>();
  if (lane == 0) {
    unsigned old = __hip_atomic_fetch_add((LAS unsigned*)(size_t)cnt, 1u, __ATOMIC_RELAXED, __HIP_MEMORY_SCOPE_WORKGROUP);
    if (old == n - 1) set_flag(ws, idx, tag);
  }
}
#define ST(i)

template <int MT, bool SWAP>
DEVINL void gemm_compute(v4f (&acc)[MT][5], unsigned xa, unsigned wa, unsigned slot_bytes, bool do_uflag, char* ws, unsigned uidx, u64 tag, int tid) {
  v4u xp[MT], wp[5], xq[MT], wq[5];
  unsigned so = slot_bytes;
#define RD(X, W, hs) do { _Pragma("unroll") for (int n = 0; n < 5; ++n) W[n] = cat(ldtr(wa + so + (hs) * 5120 + n * 32), ldtr(wa + so + (hs) * 5120 + 2560 + n * 32)); \
    _Pragma("unroll") for (int m = 0; m < MT; ++m) X[m] = lds128(((xa + so) ^ ((hs) * 64)) + m * 2048); } while (0)
#define MM(X, W) do { _Pragma("unroll") for (int m = 0; m < MT; ++m) _Pragma("unroll") for (int n = 0; n < 5; ++n) acc[m][n] = SWAP ? mfma(W[n], X[m], acc[m][n]) : mfma(X[m], W[n], acc[m][n]); } while (0)
#define MIX() do { _Pragma("unroll") for (int i = 0; i < MT * 5; ++i) { __builtin_amdgcn_sched_group_barrier(0x100, MT == 4 ? 1 : 2, 0); __builtin_amdgcn_sched_group_barrier(0x8, 1, 0); } } while (0)
  bar(); SCHED();
  RD(xp, wp, 0);
  SCHED();
#pragma unroll 1
  for (int s = 0; s < 9; ++s) {
    RD(xq, wq, 1); MM(xp, wp); MIX(); SCHED();
    if (do_uflag && s == 3) waitv<0>();
    bar(); SCHED();
    if (do_uflag && s == 3 && tid == 0) set_flag(ws, uidx, tag);
    so = (so == 2 * slot_bytes) ? 0 : so + slot_bytes;
    RD(xp, wp, 0); MM(xq, wq); MIX(); SCHED();
  }
  RD(xq, wq, 1); MM(xp, wp); MIX(); SCHED();
  bar(); SCHED();
  MM(xq, wq);
#undef RD
#undef MM
#undef MIX
}

template <int MASK>
DEVINL void conv_wave(unsigned cb, unsigned dbits, char* ws, unsigned yoff, int lane) {
  constexpr int TMAX = (MASK & 128) ? 7 : (MASK & 64) ? 6 : (MASK & 32) ? 5 : 4;
  {
    v4u h0 = lds128(cb + 3 * L_CS + 32);
    v4u z = {0, 0, 0, 0};
    if (lane < 30) sts128(cb + L_UOFF + 4096 + lane * 16, z);
    else if (lane < 38) sts128(cb + ((lane - 30) >> 1) * L_CS + (lane & 1) * 16, z);
    else if (lane == 38) sts128(cb + 2 * L_CS + 32, (v4u){h0.x << 16, (h0.x >> 16) | (h0.y << 16), (h0.y >> 16) | (h0.z << 16), (h0.z >> 16) | (h0.w << 16)});
    else if (lane == 39) sts128(cb + 1 * L_CS + 32, (v4u){0, h0.x, h0.y, h0.z});
    else if (lane == 40) sts128(cb + 32, (v4u){0, h0.x << 16, (h0.x >> 16) | (h0.y << 16), (h0.y >> 16) | (h0.z << 16)});
    asm volatile("s_waitcnt lgkmcnt(0)" ::: "memory");
  }
  const int i = lane & 15, g = lane >> 4;
  unsigned ha = cb + (i & 3) * L_CS + 16 * g + 8 * (i >> 2) + 8;
  unsigned ua = cb + L_UOFF + 480 - 32 * i + 16 * g;
  v4f acc[8];
  v4u F[8], G[8], F2[8], G2[8];
#define CRD(F, G) do { _Pragma("unroll") for (int v = 0; v <= TMAX; ++v) { G[v] = lds128(ua + (7 - v) * 512); F[v] = cat(lds64(ha + v * 512), lds64(ha + v * 512 + 8)); } ha += 64; ua += 64; } while (0)
#define CMM(F, G) do { _Pragma("unroll") for (int w = 0; w <= TMAX; ++w) _Pragma("unroll") for (int t = w; t <= TMAX; ++t) if ((MASK >> t) & 1) acc[t] = mfma(F[w], G[t - w], acc[t]); } while (0)
#define CMIX() do { _Pragma("unroll") for (int j = 0; j < (MASK == 255 ? 36 : 9); ++j) { __builtin_amdgcn_sched_group_barrier(0x100, MASK == 255 ? 1 : 3, 0); __builtin_amdgcn_sched_group_barrier(0x8, 1, 0); } } while (0)
  CRD(F, G);
  {
    const int jt = 15 - i - 8 * g; v4u dg;
#pragma unroll
    for (int d = 0; d < 4; ++d) dg[d] = (jt == 2 * d) ? dbits : (jt == 2 * d + 1) ? (dbits << 16) : 0u;
#pragma unroll
    for (int t = 0; t <= TMAX; ++t) if ((MASK >> t) & 1) acc[t] = mfma(dg, G[t], (v4f){0, 0, 0, 0});
  }
  SCHED();
#pragma unroll 1
  for (int it = 0; it < 3; ++it) {
    CRD(F2, G2); CMM(F, G); CMIX(); SCHED();
    CRD(F, G); CMM(F2, G2); CMIX(); SCHED();
  }
  CRD(F2, G2); CMM(F, G); CMIX(); SCHED();
  CMM(F2, G2);
#undef CRD
#undef CMM
#undef CMIX
  const __amdgpu_buffer_rsrc_t rs = __builtin_amdgcn_make_buffer_rsrc(ws, 0, WS_BYTES, 0x00020000);
#pragma unroll
  for (int a = 0; a < 4; ++a) {
    if (!((MASK >> a) & 1)) continue;
    const int b = 7 - a;
    v2u X = pk4(acc[a]), Y = pk4(acc[b]);
    auto r0 = __builtin_amdgcn_permlane16_swap(X.x, Y.x, false, false);
    auto r1 = __builtin_amdgcn_permlane16_swap(X.y, Y.y, false, false);
    v4u o = {r0[0], r1[0], r0[1], r1[1]};
    __builtin_amdgcn_raw_buffer_store_b128(o, rs, yoff + ((g & 1) ? b : a) * 512 + 32 * i + 16 * (g >> 1), 0, 16);
  }
}

__global__ void __launch_bounds__(512, 2) fused_kernel(Params p) {
  __shared__ __attribute__((aligned(1024))) char smem[163840];
  const int tid = threadIdx.x, lane = tid & 63;
  const int wv = __builtin_amdgcn_readfirstlane(tid >> 6);
  const int b = blockIdx.x, xcd = b & 7, bi = b >> 3;
  const bool isQ = bi >= 16;
  const int rt = 4 * (xcd >> 1) + (bi & 3), g = 4 * (xcd & 1) + ((bi >> 2) & 3);
  asm volatile("" :: "v"(smem));
  ST(0);
  const u64 tag = (*(const u64*)p.ws) * 6364136223846793005ull + 1442695040888963407ull;
  const v4i rws = mk_rsrc(p.ws, WS_BYTES);
  const __amdgpu_buffer_rsrc_t bws = __builtin_amdgcn_make_buffer_rsrc(p.ws, 0, WS_BYTES, 0x00020000);
  const int l15 = lane & 15, grp = lane >> 4;
  const unsigned trl = (4 * grp + (l15 >> 2)) * 160 + 8 * (lane & 3);
  if (tid == 0) { sts32(L_CNT, 0); sts32(L_CNT + 4, 0); }
  const int lw = wv & 3;
  v2u pq[5];

  unsigned xo0 = 0, xo1 = 0, wo_[5] = {0, 0, 0, 0, 0};
  if (wv >= 4) {
    const unsigned r = lane >> 3;
    xo0 = (rt * 128 + lw * 32 + r) * 1280 + (((lane & 7) ^ (r >> 1)) << 4);
    xo1 = (xo0 ^ 64) + 8 * 1280;
#pragma unroll
    for (int j = 0; j < 5; ++j) { const unsigned ch = j * 64 + lane, rho = ch / 10, c = ch - rho * 10;
      wo_[j] = (8 * ((rho >> 2) & 3) + 4 * (rho >> 4) + (rho & 3)) * 3840 + c * 16; }
  }
  const v4i rx = mk_rsrc(p.x, 2048 * 1280), rw = mk_rsrc(p.w, 640 * 3840), rh = mk_rsrc(p.h, 640 * 4096);
  const int gr0 = (4 * xcd + (bi >> 3)) * 64, gct = bi & 7;

  auto load_h = [&](int c, int rho, unsigned cb) {
#pragma unroll
    for (int j = 0; j < 4; ++j) dma(rh, (j == 0 && lane == 0 && rho < 3) ? OOB : (unsigned)(lane * 16 + j * 1024 + 2 * rho + c * 4096 - 6), 0, cb + rho * L_CS + 32 + j * 1024);
  };
  auto load_u = [&](int c, unsigned cb) {
#pragma unroll
    for (int j = 0; j < 4; ++j) dma_sc1(rws, lane * 16, WS_UT + c * 4096 + j * 1024, cb + L_UOFF + j * 1024);
  };
  auto load_qwo = [&](int i0, int i1) {
    const v4i rq = mk_rsrc(p.q, 2048 * 1280), ro = mk_rsrc(p.wo, 640 * 1280);
#pragma unroll 1
    for (int i = i0 + lw; i < i1; i += 4) {
      if (i < 11) { const unsigned ch = i * 64 + lane, gq = ch / 81, e = ch - gq * 81, r = e / 10, c = e - r * 10;
        dma(rq, (e < 80 && gq < 8) ? (gr0 + gq * 8 + r) * 1280 + gct * 160 + c * 16 : OOB, 0, L_QT + i * 1024); }
      else { const unsigned ch = (i - 11) * 64 + lane, r = ch / 10, c = ch - r * 10;
        dma(ro, r * 1280 + gct * 160 + c * 16, 0, i * 1024); }
    }
  };

  if (!isQ) {
    const int c0 = 80 * g + 4 * rt;
    if (wv < 4) {
      v4f acc[4][5];
#pragma unroll
      for (int m = 0; m < 4; ++m)
#pragma unroll
        for (int n = 0; n < 5; ++n) acc[m][n] = (v4f){0, 0, 0, 0};
      const unsigned xa = ((wv >> 1) * 64 + l15) * 128 + ((grp ^ (l15 >> 1)) << 4);
      gemm_compute<4, false>(acc, xa, 16384 + (wv & 1) * 10240 + trl, L_KVSLOT, false, p.ws, 0, tag, tid);
      ST(1);
#pragma unroll
      for (int n = 0; n < 5; ++n)
#pragma unroll
        for (int t = 0; t < 2; ++t) sts128((((wv & 1) * 5 + n) * 4 + (wv >> 1) * 2 + t) * 1024 + lane * 16, cat(pk4(acc[2 * t][n]), pk4(acc[2 * t + 1][n])));
    } else {
      const unsigned wcol = (640 + 640 * (lw & 1) + 80 * g) * 2 + (lw >> 1) * 32 * 3840;
      const unsigned wl = 16384 + (lw & 1) * 10240 + (lw >> 1) * 5120;
      auto issue = [&](int s, unsigned so) {
#pragma unroll
        for (int j = 0; j < 5; ++j) dma(rw, wo_[j], wcol + s * 64 * 3840, so + wl + j * 1024);
#pragma unroll
        for (int j = 0; j < 4; ++j) dma(rx, (j & 1) ? xo1 : xo0, s * 128 + (j >> 1) * 16 * 1280, so + lw * 4096 + j * 1024);
      };
      issue(0, L_KVSLOT); issue(1, 2 * L_KVSLOT); issue(2, 0);
      waitv<18>(); bar();
      unsigned so = L_KVSLOT;
#pragma unroll 1
      for (int s = 1; s < 8; ++s) { waitv<9>(); bar(); issue(s + 2, so); so = (so == 2 * L_KVSLOT) ? 0 : so + L_KVSLOT; }
      waitv<9>(); bar();
#pragma unroll 1
      for (int rho = 0; rho < 4; ++rho) load_h(c0 + lw, rho, L_KVCONV + lw * L_CH);
      waitv<16>(); bar();
      bar();
    }
    bar();
    if (wv < 5) {
      const int a = wv;
      v4f d0 = {0, 0, 0, 0}, d1 = d0, d2 = d0, dz = d0;
      const v4u ones = {0x3f803f80u, 0x3f803f80u, 0x3f803f80u, 0x3f803f80u};
      const unsigned ka = a * 4096 + lane * 16;
      const unsigned vm = (5 + (a > 0 ? a - 1 : 0)) * 4096 + lane * 16, vc = (5 + a) * 4096 + lane * 16, vp = (5 + (a < 4 ? a + 1 : 4)) * 4096 + lane * 16;
#pragma unroll
      for (int t = 0; t < 4; ++t) {
        v4u kf = lds128(ka + t * 1024);
        v4u pk = {phi2(kf.x), phi2(kf.y), phi2(kf.z), phi2(kf.w)};
        d0 = mfma(pk, lds128(vm + t * 1024), d0); d1 = mfma(pk, lds128(vc + t * 1024), d1); d2 = mfma(pk, lds128(vp + t * 1024), d2); dz = mfma(pk, ones, dz);
      }
      const int Dq = 4 * a + grp, hh = Dq / 5;
      const unsigned pb = WS_PART + (g * 16 + rt) * PART_STRIDE + Dq * 168;
      const int e0 = 16 * (a - 1) + l15 - 20 * hh;
      __builtin_amdgcn_raw_buffer_store_b64(pk4(d0), bws, ((unsigned)e0 < 20u) ? pb + e0 * 8 : OOB, 0, 16);
      __builtin_amdgcn_raw_buffer_store_b64(pk4(d1), bws, ((unsigned)(e0 + 16) < 20u) ? pb + (e0 + 16) * 8 : OOB, 0, 16);
      __builtin_amdgcn_raw_buffer_store_b64(pk4(d2), bws, ((unsigned)(e0 + 32) < 20u) ? pb + (e0 + 32) * 8 : OOB, 0, 16);
      __builtin_amdgcn_raw_buffer_store_b64(pk4(dz), bws, (l15 == 0) ? pb + 160 : OOB, 0, 16);
      ST(2);
      arrive_flag(L_CNT, 5, p.ws, FL_P + g * 16 + rt, tag, lane);
      ST(3);
    } else if (wv == 7) {
      wait_flags(p.ws, FL_U + g * 16 + l15, tag);
#pragma unroll 1
      for (int c = 0; c < 4; ++c) load_u(c0 + c, L_KVCONV + c * L_CH);
    }
    waitv<0>(); bar();
    ST(4);
    if (wv < 4) {
      conv_wave<255>(L_KVCONV + wv * L_CH, p.Dv[c0 + wv], p.ws, WS_YT + (c0 + wv) * 4096, lane);
      ST(5);
      waitv<0>();
    } else load_qwo(0, 72);
    bar();
    if (tid == 0) set_flag(p.ws, FL_Y + g * 32 + rt, tag);
    if (wv >= 4) { load_qwo(72, 111); waitv<0>(); }
    bar();
  } else {
    const int iq = bi - 16, tt = 2 * xcd + (iq >> 3), cg = iq & 7, t0 = tt * 128;
    const int c0 = 80 * g + 64 + rt;
    {
      const v4i rk = mk_rsrc(p.k, 2048 * 1280), rv = mk_rsrc(p.v, 2048 * 1280);
#pragma unroll 1
      for (int i = wv; i < 43; i += 8) {
        const bool isv = i >= 22;
        const unsigned ch = (isv ? i - 22 : i) * 64 + lane, gq = ch / 81, e = ch - gq * 81, r = e / 10, c = e - r * 10;
        const int row = t0 + (int)(gq * 8 + r) - (isv ? 0 : 8);
        const bool ok = e < 80 && row >= t0 - 3 && row >= 0 && row < t0 + 128;
        const unsigned vo = ok ? row * 1280 + cg * 160 + c * 16 : OOB;
        if (isv) dma(rv, vo, 0, L_QKIMG + i * 1024); else dma(rk, vo, 0, L_QKIMG + i * 1024);
      }
    }
    if (wv < 4) {
      waitv<0>(); bar();
      ST(1);
#pragma unroll 1
      for (int wu = wv; wu < 10; wu += 4) {
        const int tg = 8 * (wu & 1) + (lane & 7), cp = 8 * (wu >> 1) + (lane >> 3), c = 80 * cg + 2 * cp;
        const v4u sw = *(const v4u*)(p.sk + c * 4);
        const unsigned ka = L_QKIMG + tg * 1296 + cp * 4, va = L_QVIMG + tg * 1296 + cp * 4;
        float k0[11], k1[11];
#pragma unroll
        for (int j = 0; j < 11; ++j) { const unsigned u = lds32(ka + (j < 3 ? (5 + j) * 160 : 1296 + (j - 3) * 160)); k0[j] = blo(u); k1[j] = bhi(u); }
        float u0[8], u1[8];
#pragma unroll
        for (int j = 0; j < 8; ++j) { const unsigned u = lds32(va + j * 160);
          u0[j] = (k0[j] * bhi(sw.y) + k0[j + 1] * blo(sw.y) + k0[j + 2] * bhi(sw.x) + k0[j + 3] * blo(sw.x)) * blo(u);
          u1[j] = (k1[j] * bhi(sw.w) + k1[j + 1] * blo(sw.w) + k1[j + 2] * bhi(sw.z) + k1[j + 3] * blo(sw.z)) * bhi(u); }
        const unsigned uo = WS_UT + c * 4096 + 2 * (2040 - t0 - 8 * tg);
        __builtin_amdgcn_raw_buffer_store_b128((v4u){pk2(u0[7], u0[6]), pk2(u0[5], u0[4]), pk2(u0[3], u0[2]), pk2(u0[1], u0[0])}, bws, uo, 0, 16);
        __builtin_amdgcn_raw_buffer_store_b128((v4u){pk2(u1[7], u1[6]), pk2(u1[5], u1[4]), pk2(u1[3], u1[2]), pk2(u1[1], u1[0])}, bws, uo + 4096, 0, 16);
      }
      ST(2);
      v4f acc[2][5];
#pragma unroll
      for (int m = 0; m < 2; ++m)
#pragma unroll
        for (int n = 0; n < 5; ++n) acc[m][n] = (v4f){0, 0, 0, 0};
      const unsigned xa = (wv * 32 + l15) * 128 + ((grp ^ (l15 >> 1)) << 4);
      gemm_compute<2, true>(acc, xa, 16384 + trl, L_QSLOT, true, p.ws, FL_U + cg * 16 + tt, tag, tid);
      ST(3);
#pragma unroll
      for (int n = 0; n < 5; ++n) { pq[n] = pk4(acc[0][n]); sts64(L_QX + wv * 2560 + n * 512 + lane * 8, pk4(acc[1][n])); }
      bar();
    } else {
      const unsigned wcol = 160 * g + (lw >> 1) * 32 * 3840;
      const unsigned wl = 16384 + (lw >> 1) * 5120;
      const unsigned w0 = (lw & 1) ? wo_[1] : wo_[0], w1 = (lw & 1) ? wo_[3] : wo_[2], w2 = (lw & 1) ? OOB : wo_[4];
      auto issue = [&](int s, unsigned so) {
        dma(rw, w0, wcol + s * 64 * 3840, so + wl + (lw & 1) * 1024);
        dma(rw, w1, wcol + s * 64 * 3840, so + wl + (lw & 1) * 1024 + 2048);
        dma(rw, w2, wcol + s * 64 * 3840, (lw & 1) ? L_DUMMY : so + wl + 4096);
#pragma unroll
        for (int j = 0; j < 4; ++j) dma(rx, (j & 1) ? xo1 : xo0, s * 128 + (j >> 1) * 16 * 1280, so + lw * 4096 + j * 1024);
      };
      issue(0, L_QSLOT); issue(1, 2 * L_QSLOT); issue(2, 0);
      waitv<21>(); bar();
      waitv<14>(); bar();
      unsigned so = L_QSLOT;
#pragma unroll 1
      for (int s = 1; s < 8; ++s) { waitv<7>(); bar(); issue(s + 2, so); so = (so == 2 * L_QSLOT) ? 0 : so + L_QSLOT; }
      waitv<7>(); bar();
      load_h(c0, lw, L_QCONV);
      waitv<4>(); bar();
      bar();
      if (wv == 7) { wait_flags(p.ws, FL_U + g * 16 + l15, tag); load_u(c0, L_QCONV); }
      waitv<0>();
      bar();
#pragma unroll
      for (int n = 0; n < 5; ++n) pq[n] = lds64(L_QX + lw * 2560 + n * 512 + lane * 8);
      load_qwo(0, 111);
    }
#pragma unroll
    for (int n = 0; n < 5; ++n) pq[n] = (v2u){phi2(pq[n].x), phi2(pq[n].y)};
    ST(4);
    if (wv < 4) {
      const unsigned db = p.Dv[c0], yo = WS_YT + c0 * 4096;
      if (wv == 0) conv_wave<0x81>(L_QCONV, db, p.ws, yo, lane);
      else if (wv == 1) conv_wave<0x42>(L_QCONV, db, p.ws, yo, lane);
      else if (wv == 2) conv_wave<0x24>(L_QCONV, db, p.ws, yo, lane);
      else conv_wave<0x18>(L_QCONV, db, p.ws, yo, lane);
      ST(5);
      arrive_flag(L_CNT, 4, p.ws, FL_Y + g * 32 + 16 + rt, tag, lane);
    } else if (wv == 7) wait_flags(p.ws, FL_P + g * 16 + l15, tag);
    bar();
    ST(6);
    if (wv < 7) {
      const unsigned po = (tid < 420) ? WS_PART + g * 16 * PART_STRIDE + tid * 8 : OOB;
      v2u r[16];
#pragma unroll
      for (int j = 0; j < 16; ++j) r[j] = __builtin_amdgcn_raw_buffer_load_b64(bws, po, j * PART_STRIDE, 16);
      v4f s = {0, 0, 0, 0};
#pragma unroll
      for (int j = 0; j < 16; ++j) { s[0] += blo(r[j].x); s[1] += bhi(r[j].x); s[2] += blo(r[j].y); s[3] += bhi(r[j].y); }
      sts64(L_KVC + tid * 8, pk4(s));
    }
    waitv<0>(); bar();
    ST(7);
    {
      const v2u z2 = {0, 0};
      auto kvf = [&](int a, int bb) { const int Dq = 4 * a + grp, e = 16 * bb + l15 - 20 * (Dq / 5); return lds64(L_KVC + (((unsigned)e < 20u) ? Dq * 21 + e : 420) * 8); };
      auto zf = [&](int a) { const int Dq = 4 * a + grp; return lds64(L_KVC + ((Dq / 5 == (l15 & 3)) ? Dq * 21 + 20 : 420) * 8); };
      const v4u s0 = cat(pq[0], pq[1]), s1 = cat(pq[2], pq[3]), s2 = cat(pq[4], z2);
      const v4f zero = {0, 0, 0, 0};
      v4f den = mfma(cat(zf(0), zf(1)), s0, zero); den = mfma(cat(zf(2), zf(3)), s1, den); den = mfma(cat(zf(4), z2), s2, den);
      v4f n0 = mfma(cat(kvf(0, 0), kvf(1, 0)), s0, zero);
      v4f n1 = mfma(cat(kvf(0, 1), kvf(1, 1)), s0, zero); n1 = mfma(cat(kvf(2, 1), z2), s1, n1);
      v4f n2 = mfma(cat(z2, kvf(1, 2)), s0, zero); n2 = mfma(cat(kvf(2, 2), kvf(3, 2)), s1, n2);
      v4f n3 = mfma(cat(kvf(2, 3), kvf(3, 3)), s1, zero); n3 = mfma(cat(kvf(4, 3), z2), s2, n3);
      v4f n4 = mfma(cat(z2, kvf(3, 4)), s1, zero); n4 = mfma(cat(kvf(4, 4), z2), s2, n4);
      const float r0 = __builtin_amdgcn_rcpf(den[0] + 1e-6f), r1 = __builtin_amdgcn_rcpf(den[1] + 1e-6f), r2 = __builtin_amdgcn_rcpf(den[2] + 1e-6f), r3 = __builtin_amdgcn_rcpf(den[3] + 1e-6f);
      n0 *= r0; n1 *= (grp < 1) ? r0 : r1; n2 *= (grp < 2) ? r1 : r2; n3 *= (grp < 3) ? r2 : r3; n4 *= r3;
      const int mt = rt * 8 + 2 * lw + (wv >> 2);
      const unsigned oo = WS_OF + (mt * 8 + g) * 2560;
      __builtin_amdgcn_raw_buffer_store_b128(cat(pk4(n0), pk4(n1)), bws, oo + lane * 16, 0, 16);
      __builtin_amdgcn_raw_buffer_store_b128(cat(pk4(n2), pk4(n3)), bws, oo + 1024 + lane * 16, 0, 16);
      __builtin_amdgcn_raw_buffer_store_b64(pk4(n4), bws, oo + 2048 + lane * 8, 0, 16);
      ST(8);
      waitv<0>();
      if (lane == 0) set_flag(p.ws, FL_O + mt * 8 + g, tag);
    }
  }
  ST(9);
  const __amdgpu_buffer_rsrc_t bout = __builtin_amdgcn_make_buffer_rsrc(p.out, 0, 2048 * 2560, 0x00020000);
  if (wv < 4) {
    const int mt = (gr0 >> 4) + wv;
    wait_flags(p.ws, FL_O + mt * 8 + (lane & 7), tag);
    ST(10);
    v4u of[20];
#pragma unroll
    for (int y = 0; y < 4; ++y) {
      const unsigned oo = WS_OF + (mt * 8 + 2 * y) * 2560;
      of[5 * y + 0] = __builtin_amdgcn_raw_buffer_load_b128(bws, lane * 16, oo, 16);
      of[5 * y + 1] = __builtin_amdgcn_raw_buffer_load_b128(bws, lane * 16 + 1024, oo, 16);
      of[5 * y + 2] = __builtin_amdgcn_raw_buffer_load_b128(bws, lane * 16 + 2560, oo, 16);
      of[5 * y + 3] = __builtin_amdgcn_raw_buffer_load_b128(bws, lane * 16 + 3584, oo, 16);
      of[5 * y + 4] = cat(__builtin_amdgcn_raw_buffer_load_b64(bws, lane * 8 + 2048, oo, 16), __builtin_amdgcn_raw_buffer_load_b64(bws, lane * 8 + 2048, oo + 2560, 16));
    }
    v4f acc[5];
#pragma unroll
    for (int n = 0; n < 5; ++n) acc[n] = (v4f){0, 0, 0, 0};
#pragma unroll
    for (int y = 0; y < 4; ++y) {
      const unsigned wb = L_WO + trl + y * 25600;
#pragma unroll
      for (int s = 0; s < 5; ++s) {
        const unsigned ra = (s == 4) ? 64 : (s >> 1) * 80 + (s & 1) * 32, rb = (s == 4) ? 144 : ra + 16;
#pragma unroll
        for (int n = 0; n < 5; ++n) acc[n] = mfma(cat(ldtr(wb + ra * 160 + n * 32), ldtr(wb + rb * 160 + n * 32)), of[5 * y + s], acc[n]);
      }
    }
    const unsigned ob = (gr0 + wv * 16 + l15) * 2560 + 1280 + gct * 160 + grp * 8;
#pragma unroll
    for (int n = 0; n < 5; ++n) __builtin_amdgcn_raw_buffer_store_b64(pk4(acc[n]), bout, ob + n * 32, 0, 2);
    ST(11);
    if (b == 0 && wv == 0) {
      wait_flags(p.ws, FL_U + lane, tag); wait_flags(p.ws, FL_U + 64 + lane, tag);
      if (lane == 0) __hip_atomic_store((GAS u64*)p.ws, tag, RLX_AGENT);
    }
  } else {
    wait_flags(p.ws, FL_Y + gct * 32 + (lane & 31), tag);
    ST(10);
#pragma unroll 1
    for (int cb = lw; cb < 5; cb += 4) {
      const int tg = lane & 7, cp = 8 * cb + (lane >> 3);
      const unsigned yo = WS_YT + (80 * gct + 2 * cp) * 4096 + 2 * (gr0 + 8 * tg);
      const v4u y0 = __builtin_amdgcn_raw_buffer_load_b128(bws, yo, 0, 16), y1 = __builtin_amdgcn_raw_buffer_load_b128(bws, yo, 4096, 16);
      const unsigned qa = L_QT + tg * 1296 + cp * 4;
#pragma unroll
      for (int j = 0; j < 8; ++j) { const unsigned u = lds32(qa + j * 160);
        const float a0 = (j & 1) ? bhi(y0[j >> 1]) : blo(y0[j >> 1]), a1 = (j & 1) ? bhi(y1[j >> 1]) : blo(y1[j >> 1]);
        sts32(qa + j * 160, pk2(blo(u) * a0, bhi(u) * a1)); }
      asm volatile("s_waitcnt lgkmcnt(0)" ::: "memory");
#pragma unroll
      for (int j = 0; j < 2; ++j) { const int row = j * 32 + (lane >> 1);
        const v4u o = lds128(L_QT + (row >> 3) * 1296 + (row & 7) * 160 + cb * 32 + (lane & 1) * 16);
        __builtin_amdgcn_raw_buffer_store_b128(o, bout, (gr0 + row) * 2560 + gct * 160 + cb * 32 + (lane & 1) * 16, 0, 2); }
    }
    ST(11);
  }
}

__global__ void tinyA(char* ws){ if (threadIdx.x==0) *(u64*)(ws+2048)=__builtin_amdgcn_s_memrealtime(); }
__global__ void tinyC(char* ws){ u64 until=*(volatile u64*)(ws+2048)+10000; u64 now=__builtin_amdgcn_s_memrealtime(); if (until>now+10000) until=now; while(__builtin_amdgcn_s_memrealtime()<until) __builtin_amdgcn_s_sleep(10); }
extern "C" void kernel_launch(void* const* d_in, const int* in_sizes, int n_in, void* d_out, int out_size, void* d_ws, size_t ws_size, hipStream_t stream) {
  Params p;
  p.q = (const u16*)d_in[0]; p.k = (const u16*)d_in[1]; p.v = (const u16*)d_in[2]; p.sk = (const u16*)d_in[3]; p.h = (const u16*)d_in[4];
  p.Dv = (const u16*)d_in[5]; p.x = (const u16*)d_in[6]; p.w = (const u16*)d_in[7]; p.wo = (const u16*)d_in[8];
  p.out = (u16*)d_out; p.ws = (char*)d_ws;
  tinyA<<<1,64,0,stream>>>(p.ws);
  fused_kernel<<<256, 512, 0, stream>>>(p);
}
```

```cpp
#include <hip/hip_runtime.h>
typedef unsigned short u16;
typedef unsigned long long u64;
typedef unsigned v2u __attribute__((ext_vector_type(2)));
typedef unsigned v4u __attribute__((ext_vector_type(4)));
typedef int v4i __attribute__((ext_vector_type(4)));
typedef float v2f __attribute__((ext_vector_type(2)));
typedef float v4f __attribute__((ext_vector_type(4)));
typedef short v4s __attribute__((ext_vector_type(4)));
typedef __bf16 v2bf __attribute__((ext_vector_type(2)));
typedef __bf16 v8bf __attribute__((ext_vector_type(8)));
#define GAS __attribute__((address_space(1)))
#define LAS __attribute__((address_space(3)))
#define RLX_AGENT __ATOMIC_RELAXED, __HIP_MEMORY_SCOPE_AGENT
#define DEVINL __device__ __forceinline__

constexpr unsigned WS_FLAG = 4096;
constexpr unsigned FL_U = 0, FL_P = 128, FL_Y = 256, FL_O = 512;
constexpr unsigned WS_UT = 1u << 20, WS_YT = 4u << 20, WS_PART = 8u << 20, WS_OF = 9u << 20, WS_STAMP = 12u << 20;
constexpr unsigned WS_BYTES = 13u << 20;
constexpr unsigned PART_STRIDE = 3456;
constexpr unsigned OOB = 0x80000000u;
constexpr unsigned L_KVSLOT = 36864, L_QSLOT = 26624;
constexpr unsigned L_CS = 4160, L_UOFF = 4 * L_CS, L_CH = 21248;
constexpr unsigned L_KVCONV = 73728, L_QKIMG = 79872, L_QVIMG = 79872 + 9 * 2576, L_QCONV = 123904;
constexpr unsigned L_QX = 145408, L_KVC = 155648, L_PC = 158720, L_DUMMY = 162816, L_CNT = 163584;
constexpr unsigned L_QT = 0, L_WO = 11264, L_SW = 161280;
#define CODE_BYTES 35000

struct Params { const u16 *q, *k, *v, *sk, *h, *Dv, *x, *w, *wo; u16* out; char* ws; };

DEVINL v4i mk_rsrc(const void* p, unsigned bytes) {
  u64 a = (u64)p; v4i r; r.x = (int)(unsigned)a; r.y = (int)((unsigned)(a >> 32) & 0xffffu); r.z = (int)bytes; r.w = 0x00020000; return r;
}
DEVINL void dma(v4i rs, unsigned voff, unsigned soff, unsigned lds) {
  asm volatile("s_mov_b32 m0, %3\n\ts_nop 0\n\tbuffer_load_dwordx4 %0, %1, %2 offen lds" :: "v"(voff), "s"(rs), "s"(soff), "s"(lds) : "memory");
}
DEVINL void dma_sc1(v4i rs, unsigned voff, unsigned soff, unsigned lds) {
  asm volatile("s_mov_b32 m0, %3\n\ts_nop 0\n\tbuffer_load_dwordx4 %0, %1, %2 offen sc1 lds" :: "v"(voff), "s"(rs), "s"(soff), "s"(lds) : "memory");
}
DEVINL void warm(v4i rs, unsigned voff, unsigned soff) {
  asm volatile("s_mov_b32 m0, %3\n\ts_nop 0\n\tbuffer_load_dword %0, %1, %2 offen lds" :: "v"(voff), "s"(rs), "s"(soff), "s"(L_DUMMY) : "memory");
}
template <int N> DEVINL void waitv() { asm volatile("s_waitcnt vmcnt(%0)" :: "n"(N) : "memory"); }
DEVINL void rawbar() { asm volatile("s_barrier" ::: "memory"); }
DEVINL void bar() { asm volatile("s_waitcnt lgkmcnt(0)\n\ts_barrier" ::: "memory"); }
#define SCHED() __builtin_amdgcn_sched_barrier(0)
DEVINL v4u lds128(unsigned a) { return *(const LAS v4u*)(size_t)a; }
DEVINL v2u lds64(unsigned a) { return *(const LAS v2u*)(size_t)a; }
DEVINL unsigned lds32(unsigned a) { return *(const LAS unsigned*)(size_t)a; }
DEVINL void sts128(unsigned a, v4u v) { *(LAS v4u*)(size_t)a = v; }
DEVINL void sts64(unsigned a, v2u v) { *(LAS v2u*)(size_t)a = v; }
DEVINL void sts32(unsigned a, unsigned v) { *(LAS unsigned*)(size_t)a = v; }
DEVINL v2u ldtr(unsigned a) { return __builtin_bit_cast(v2u, __builtin_amdgcn_ds_read_tr16_b64_v4i16((LAS v4s*)(size_t)a)); }
DEVINL v4u cat(v2u a, v2u b) { return (v4u){a.x, a.y, b.x, b.y}; }
DEVINL v4f mfma(v4u a, v4u b, v4f c) { return __builtin_amdgcn_mfma_f32_16x16x32_bf16(__builtin_bit_cast(v8bf, a), __builtin_bit_cast(v8bf, b), c, 0, 0, 0); }
DEVINL unsigned pk2(float a, float b) { v2f p = {a, b}; return __builtin_bit_cast(unsigned, __builtin_convertvector(p, v2bf)); }
DEVINL v2u pk4(v4f a) { return (v2u){pk2(a[0], a[1]), pk2(a[2], a[3])}; }
DEVINL float blo(unsigned u) { return __uint_as_float(u << 16); }
DEVINL float bhi(unsigned u) { return __uint_as_float(u & 0xffff0000u); }
DEVINL unsigned phi2(unsigned u) {
  const v2f t = {blo(u), bhi(u)};
  const v2f a = t * 1.44269504089f, c = t + 1e-6f;
  const v2f e = {__builtin_amdgcn_fmed3f(__builtin_amdgcn_exp2f(a.x), 0.f, 1.f), __builtin_amdgcn_fmed3f(__builtin_amdgcn_exp2f(a.y), 0.f, 1.f)};
  const v2f m = {fmaxf(c.x, 1e-6f), fmaxf(c.y, 1e-6f)};
  const v2f r = m + e;
  return pk2(r.x, r.y);
}
DEVINL v2u phi4(v4f c) {
  const v4f a = c * 1.44269504089f - 1.44269504089e-6f;
  v4f r;
#pragma unroll
  for (int j = 0; j < 4; ++j) r[j] = __int_as_float(max(__float_as_int(c[j]), 0x358637bd));
  const v4f e = {__builtin_amdgcn_fmed3f(__builtin_amdgcn_exp2f(a[0]), 0.f, 1.f), __builtin_amdgcn_fmed3f(__builtin_amdgcn_exp2f(a[1]), 0.f, 1.f),
                 __builtin_amdgcn_fmed3f(__builtin_amdgcn_exp2f(a[2]), 0.f, 1.f), __builtin_amdgcn_fmed3f(__builtin_amdgcn_exp2f(a[3]), 0.f, 1.f)};
  return pk4(r + e);
}
#define PINV(x) asm volatile("" : "+v"(x))
#define PINS(x) asm volatile("" : "+s"(x))

DEVINL void set_flag(char* ws, unsigned idx, u64 tag) { __hip_atomic_store((GAS u64*)(ws + WS_FLAG + idx * 128u), tag, RLX_AGENT); }
DEVINL void wait_flags(char* ws, unsigned idx, u64 tag) {
  GAS u64* p = (GAS u64*)(ws + WS_FLAG + idx * 128u);
#pragma unroll 1
  for (unsigned spins = 0; spins < (1u << 16); ++spins) {
    u64 f = __hip_atomic_load(p, RLX_AGENT);
    if (__all(f == tag)) break;
    __builtin_amdgcn_s_sleep(1);
  }
  __builtin_amdgcn_fence(__ATOMIC_ACQUIRE, "wavefront");
  asm volatile("" ::: "memory");
}
DEVINL void arrive_flag(unsigned cnt, unsigned n, char* ws, unsigned idx, u64 tag, int lane) {
  waitv<0>();
  if (lane == 0) {
    unsigned old = __hip_atomic_fetch_add((LAS unsigned*)(size_t)cnt, 1u, __ATOMIC_RELAXED, __HIP_MEMORY_SCOPE_WORKGROUP);
    if (old == n - 1) set_flag(ws, idx, tag);
  }
}
#define ST(i)
#define STC(i)
#define STARG
#define STPASS

DEVINL void gemm_kv(v4f (&acc)[4][5], unsigned xa, unsigned wa, unsigned wb STARG) {
  v4u xp[4], wp[5], xq[4], wq[5];
  unsigned so = 0;
#define WAD(n) ((n) == 2 ? wb + so : wa + so + ((n) > 2 ? 10240 + ((n) - 3) * 32 : (n) * 32))
#define RD(X, W, hs) do { X[0] = lds128((xa + so) ^ ((hs) * 64)); \
    _Pragma("unroll") for (int n = 0; n < 5; ++n) W[n] = cat(ldtr(WAD(n) + (hs) * 5120), ldtr(WAD(n) + (hs) * 5120 + 2560)); \
    _Pragma("unroll") for (int m = 1; m < 4; ++m) X[m] = lds128(((xa + so) ^ ((hs) * 64)) + m * 2048); } while (0)
#define MM(X, W) do { _Pragma("unroll") for (int m = 0; m < 4; ++m) _Pragma("unroll") for (int n = 0; n < 5; ++n) acc[m][n] = mfma(X[m], W[n], acc[m][n]); } while (0)
#define MIX() do { _Pragma("unroll") for (int i = 0; i < 20; ++i) { __builtin_amdgcn_sched_group_barrier(0x8, 1, 0); __builtin_amdgcn_sched_group_barrier(0x100, 1, 0); } } while (0)
  bar(); SCHED();
  RD(xp, wp, 0);
  SCHED();
#pragma unroll 1
  for (int s = 0; s < 9; ++s) {
    RD(xq, wq, 1); MM(xp, wp); MIX(); SCHED();
    rawbar(); SCHED();
    so = (so == 3 * L_KVSLOT) ? 0 : so + L_KVSLOT;
    RD(xp, wp, 0); MM(xq, wq); MIX(); SCHED();
  }
  RD(xq, wq, 1); MM(xp, wp); MIX(); SCHED();
  bar(); SCHED();
  ST(1);
#pragma unroll
  for (int n = 0; n < 5; ++n)
#pragma unroll
    for (int m = 0; m < 4; ++m) acc[m][n] = mfma(xq[m], wq[n], acc[m][n]);
#undef WAD
#undef RD
#undef MM
#undef MIX
}
DEVINL void gemm_q(v4f (&acc)[2][5], unsigned xa, unsigned wa) {
  v4u xp[2][2], wp[2][5], xq[2][2], wq[2][5];
  unsigned so = 0;
#define RD(X, W) do { _Pragma("unroll") for (int hs = 0; hs < 2; ++hs) { X[hs][0] = lds128((xa + so) ^ (hs * 64)); \
    _Pragma("unroll") for (int n = 0; n < 5; ++n) W[hs][n] = cat(ldtr(wa + so + hs * 5120 + n * 32), ldtr(wa + so + hs * 5120 + 2560 + n * 32)); \
    X[hs][1] = lds128(((xa + so) ^ (hs * 64)) + 2048); } so = (so == 3 * L_QSLOT) ? 0 : so + L_QSLOT; } while (0)
#define MM(X, W) do { _Pragma("unroll") for (int hs = 0; hs < 2; ++hs) _Pragma("unroll") for (int m = 0; m < 2; ++m) _Pragma("unroll") for (int n = 0; n < 5; ++n) \
    acc[m][n] = mfma(W[hs][n], X[hs][m], acc[m][n]); } while (0)
#define MIX() do { _Pragma("unroll") for (int i = 0; i < 7; ++i) { __builtin_amdgcn_sched_group_barrier(0x100, 2, 0); __builtin_amdgcn_sched_group_barrier(0x8, 1, 0); \
    __builtin_amdgcn_sched_group_barrier(0x100, 1, 0); __builtin_amdgcn_sched_group_barrier(0x8, 1, 0); __builtin_amdgcn_sched_group_barrier(0x100, 1, 0); __builtin_amdgcn_sched_group_barrier(0x8, 1, 0); } } while (0)
  bar(); SCHED();
  RD(xp, wp);
  SCHED();
#pragma unroll 1
  for (int s = 0; s < 4; ++s) {
    rawbar(); SCHED();
    RD(xq, wq); MM(xp, wp); MIX(); SCHED();
    rawbar(); SCHED();
    RD(xp, wp); MM(xq, wq); MIX(); SCHED();
  }
  rawbar(); SCHED();
  RD(xq, wq); MM(xp, wp); MIX(); SCHED();
  bar(); SCHED();
  MM(xq, wq);
#undef RD
#undef MM
#undef MIX
}

template <int MASK>
DEVINL void conv_wave(unsigned cb, unsigned dbits, char* ws, unsigned yoff, int lane STARG) {
  constexpr int TMAX = (MASK & 128) ? 7 : (MASK & 64) ? 6 : (MASK & 32) ? 5 : 4;
  const int i = lane & 15, g = lane >> 4;
  unsigned ha = cb + (i >> 2) * L_CS + 16 * g + 8 * (i & 3) + 8;
  unsigned ua = cb + L_UOFF + 480 - 32 * i + 16 * g;
  PINV(ha); PINV(ua);
  v4f acc[8];
  v4u F[8], G[8], F2[8], G2[8];
#define CRD(F, G) do { _Pragma("unroll") for (int v = 0; v <= TMAX; ++v) { G[v] = lds128(ua + (7 - v) * 512); F[v] = cat(ldtr(ha + v * 512), ldtr(ha + v * 512 + 8)); } ha += 64; ua += 64; PINV(ha); PINV(ua); } while (0)
#define CMM(F, G) do { _Pragma("unroll") for (int w = 0; w <= TMAX; ++w) _Pragma("unroll") for (int t = w; t <= TMAX; ++t) if ((MASK >> t) & 1) acc[t] = mfma(F[w], G[t - w], acc[t]); } while (0)
#define CMIX() do { _Pragma("unroll") for (int j = 0; j < (MASK == 255 ? 36 : 9); ++j) { __builtin_amdgcn_sched_group_barrier(0x100, MASK == 255 ? 1 : 3, 0); __builtin_amdgcn_sched_group_barrier(0x8, 1, 0); } } while (0)
  CRD(F, G);
  {
    const int jt = 15 - i - 8 * g; v4u dg;
#pragma unroll
    for (int d = 0; d < 4; ++d) dg[d] = (jt == 2 * d) ? dbits : (jt == 2 * d + 1) ? (dbits << 16) : 0u;
#pragma unroll
    for (int t = 0; t <= TMAX; ++t) if ((MASK >> t) & 1) acc[t] = mfma(dg, G[t], (v4f){0, 0, 0, 0});
  }
  SCHED(); ST(16);
#pragma unroll 1
  for (int it = 0; it < 3; ++it) {
    CRD(F2, G2); CMM(F, G); CMIX(); SCHED();
    CRD(F, G); CMM(F2, G2); CMIX(); SCHED();
  }
  ST(17);
  CRD(F2, G2); CMM(F, G); CMIX(); SCHED();
  if (MASK == 255) { bar(); SCHED(); }
  CMM(F2, G2);
#undef CRD
#undef CMM
#undef CMIX
  const __amdgpu_buffer_rsrc_t rs = __builtin_amdgcn_make_buffer_rsrc(ws, 0, WS_BYTES, 0x00020000);
#pragma unroll
  for (int a = 0; a < 4; ++a) {
    if (!((MASK >> a) & 1)) continue;
    const int b = 7 - a;
    v2u X = pk4(acc[a]), Y = pk4(acc[b]);
    auto r0 = __builtin_amdgcn_permlane16_swap(X.x, Y.x, false, false);
    auto r1 = __builtin_amdgcn_permlane16_swap(X.y, Y.y, false, false);
    v4u o = {r0[0], r1[0], r0[1], r1[1]};
    __builtin_amdgcn_raw_buffer_store_b128(o, rs, yoff + ((g & 1) ? b : a) * 512 + 32 * i + 16 * (g >> 1), 0, 16);
  }
}

__global__ void __launch_bounds__(512, 2) fused_kernel(Params p) {
  __shared__ __attribute__((aligned(1024))) char smem[163840];
  u64 pc; asm volatile("s_getpc_b64 %0" : "=s"(pc));
  const int tid = threadIdx.x, lane = tid & 63;
  const int b = blockIdx.x, xcd = b & 7, bi = b >> 3;
  warm(mk_rsrc((const void*)pc, CODE_BYTES), tid < 36 ? ((bi & 7) * 36 + tid) * 128 : OOB, 0);
  const int wv = __builtin_amdgcn_readfirstlane(tid >> 6);
  const bool isQ = bi >= 16;
  const int rt = 4 * (xcd >> 1) + (bi & 3), g = 4 * (xcd & 1) + ((bi >> 2) & 3);
  ST(0);
  typedef __attribute__((address_space(4))) const u64 cu64;
  typedef __attribute__((address_space(4))) const unsigned cu32;
  const int lw = wv & 3, iq = bi & 15;
  int k0 = ((xcd >> 1) * 5 >> 1) + 5 * (xcd & 1); k0 = k0 >= 10 ? k0 - 10 : k0;
  v4i rx, rw;
#define MK_XW() do { const u16 *x_ = p.x, *w_ = p.w; PINS(x_); PINS(w_); rx = mk_rsrc(x_, 2048 * 1280); rw = mk_rsrc(w_, 640 * 3840); } while (0)
  auto off_x = [&](int j) {
    int ka = k0 + 2 * j + (lane >> 5); ka = ka >= 10 ? ka - 10 : ka;
    unsigned vo = ((xcd >> 1) * 512 + iq * 32 + (lane & 31)) * 1280 + ka * 128; PINV(vo);
    return vo;
  };
  auto off_w = [&](int j) {
    const unsigned r10 = lane / 10, c10 = lane - r10 * 10, rho = 6 * j + r10; int kb = k0 + (rho >> 2); kb = kb >= 10 ? kb - 10 : kb;
    unsigned vo = (kb * 64 + iq * 4 + (rho & 3)) * 3840 + 1280 + (xcd & 1) * 640 + c10 * 128 + (c10 >= 5 ? 640 : 0); PINV(vo);
    vo = (lane < 60 && rho < 40) ? vo : OOB; PINV(vo);
    return vo;
  };
  auto warm_x = [&](int j) { warm(rx, off_x(j), 0); };
  auto warm_w = [&](int j) { warm(rw, off_w(j), 0); };
  unsigned xo0 = 0, xo1 = 0, wo_[5] = {0, 0, 0, 0, 0};
  auto loader_offsets = [&]() {
    const unsigned r = lane >> 3;
    xo0 = (rt * 128 + lw * 32 + r) * 1280 + (((lane & 7) ^ (r >> 1)) << 4);
    xo1 = (xo0 ^ 64) + 8 * 1280;
#pragma unroll
    for (int j = 0; j < 5; ++j) { const unsigned ch = j * 64 + lane, rho = ch / 10, c = ch - rho * 10;
      wo_[j] = (8 * ((rho >> 2) & 3) + 4 * (rho >> 4) + (rho & 3)) * 3840 + c * 16; PINV(wo_[j]); }
    PINV(xo0); PINV(xo1);
  };
  const unsigned kwcol = (640 + 640 * (lw & 1) + 80 * g) * 2 + (lw >> 1) * 32 * 3840, kwl = 16384 + (lw & 1) * 10240 + (lw >> 1) * 5120;
  auto issue_x = [&](int ks, unsigned so) {
#pragma unroll
    for (int j = 0; j < 4; ++j) dma(rx, (j & 1) ? xo1 : xo0, ks * 128 + (j >> 1) * 16 * 1280, so + lw * 4096 + j * 1024);
  };
  auto issue_kw = [&](int ks, unsigned so) {
#pragma unroll
    for (int j = 0; j < 5; ++j) dma(rw, wo_[j], kwcol + ks * 64 * 3840, so + kwl + j * 1024);
  };
  const int tt = 4 * xcd + (iq >> 2), cq = iq & 3, t0 = tt * 64;
  if (isQ) {
    const unsigned r20 = lane / 20, so = (t0 + wv * 8) * 1280, la = L_QKIMG + 2576 + wv * 2576;
    unsigned vo = r20 * 1280 + (lane - r20 * 20) * 16 + cq * 320, xv = off_x(1); PINV(vo);
    MK_XW();
    if (wv == 0) warm(rx, xv, 0);
    const v4i rk = mk_rsrc(p.k, 2048 * 1280), rv = mk_rsrc(p.v, 2048 * 1280);
    if (lane < 60) {
      dma(rk, vo, so, la); dma(rk, vo, so + 3840, la + 960); dma(rv, vo, so, la + 8 * 2576); dma(rv, vo, so + 3840, la + 8 * 2576 + 960);
      if (wv == 7) dma(rk, tt ? vo + (t0 - 3) * 1280 : OOB, 0, L_QKIMG + 1600);
      if (lane < 40) {
        dma(rk, vo, so + 7680, la + 1920); dma(rv, vo, so + 7680, la + 8 * 2576 + 1920);
        if (wv == 5 || wv == 6) dma(mk_rsrc(p.sk, 640 * 8), cq * 1280 + (wv - 5) * 640 + lane * 16, 0, L_SW + (wv - 5) * 640);
      }
    }
  } else if (wv >= 4) { loader_offsets(); MK_XW(); issue_x(k0, 0); issue_kw(k0, 0); }
  else { const unsigned xv = off_x(0), wz = off_w(wv - 1); MK_XW(); warm(wv == 0 ? rx : rw, wv == 0 ? xv : wv < 3 ? wz : OOB, 0); }
  ST(12);
  asm volatile("" ::: "memory");
  const int c0 = isQ ? 80 * g + 64 + rt : 80 * g + 4 * rt + lw;
  const v4i rws = mk_rsrc(p.ws, WS_BYTES), ro = mk_rsrc(p.wo, 640 * 1280);
  const __amdgpu_buffer_rsrc_t bws = __builtin_amdgcn_make_buffer_rsrc(p.ws, 0, WS_BYTES, 0x00020000);
  const int l15 = lane & 15, grp = lane >> 4;
  const unsigned trl = (4 * grp + (l15 >> 2)) * 160 + 8 * (lane & 3);
  v2u pq[5];
  const int gr0 = (4 * xcd + (bi >> 3)) * 64, gct = bi & 7;
  const __amdgpu_buffer_rsrc_t bh = __builtin_amdgcn_make_buffer_rsrc((void*)p.h, 0, 640 * 4096, 0x00020000);

  auto h_store = [&](unsigned cb, int j, v4u d, v2u m) {
    const unsigned a = cb + 32 + j * 1024 + lane * 16;
    const unsigned e0 = __builtin_amdgcn_alignbit(m.y, m.x, 16), e1 = __builtin_amdgcn_alignbit(d.x, m.y, 16), e2 = __builtin_amdgcn_alignbit(d.y, d.x, 16),
                   e3 = __builtin_amdgcn_alignbit(d.z, d.y, 16), e4 = __builtin_amdgcn_alignbit(d.w, d.z, 16);
    sts128(a, (v4u){e0, e1, e2, e3}); sts128(a + L_CS, (v4u){m.y, d.x, d.y, d.z}); sts128(a + 2 * L_CS, (v4u){e1, e2, e3, e4}); sts128(a + 3 * L_CS, d);
  };
  auto h_pads = [&](unsigned cb) {
    if (lane < 38) sts128(lane < 30 ? cb + L_UOFF + 4096 + lane * 16 : cb + ((lane - 30) >> 1) * L_CS + (lane & 1) * 16, (v4u){0, 0, 0, 0});
  };
  auto load_u = [&](int c, unsigned cb) {
#pragma unroll
    for (int j = 0; j < 4; ++j) dma_sc1(rws, lane * 16, WS_UT + c * 4096 + j * 1024, cb + L_UOFF + j * 1024);
  };
  auto load_q = [&](int w, int nw) {
    const v4i rq = mk_rsrc(p.q, 2048 * 1280);
#pragma unroll 1
    for (int i = w; i < 11; i += nw) { const unsigned ch = i * 64 + lane, gq = ch / 81, e = ch - gq * 81, r = e / 10, c = e - r * 10;
      dma(rq, (e < 80 && gq < 8) ? (gr0 + gq * 8 + r) * 1280 + gct * 160 + c * 16 : OOB, 0, L_QT + i * 1024); }
  };
  auto load_wo = [&](int w, int nw, int i0, int i1) {
    unsigned vo[5];
#pragma unroll
    for (int j = 0; j < 5; ++j) { const unsigned ch = j * 64 + lane, r = ch / 10, c = ch - r * 10; vo[j] = r * 1280 + c * 16; }
    int gb = bi; PINS(gb);
#pragma unroll 1
    for (int i = i0 + w; i < i1; i += nw)
#pragma unroll
      for (int j = 0; j < 5; ++j) dma(ro, vo[j], i * 32 * 1280 + (gb & 7) * 160, L_WO + i * 5120 + j * 1024);
  };
  u64 tag; unsigned dbits;
#define GET_TAG() do { const char* w_ = p.ws; const u16* d_ = p.Dv; PINS(w_); PINS(d_); u64 e_ = *(cu64*)w_; dbits = ((cu32*)d_)[c0 >> 1]; PINS(e_); PINS(dbits); \
    dbits = (dbits >> (16 * (c0 & 1))) & 0xffffu; tag = e_ * 6364136223846793005ull + 1442695040888963407ull; } while (0)

  if (!isQ) {
    unsigned pa0, pa1, pa2, pa3;
    auto part_addr = [&]() {
      const int Dq = 4 * wv + grp, hh = Dq / 5, e1 = 16 * wv + l15 - 20 * hh;
      const unsigned pb = L_PC + Dq * 168, trash = L_PC + 3360;
      pa0 = (unsigned)(e1 - 16) < 20u ? pb + (e1 - 16) * 8 : trash; pa1 = (unsigned)e1 < 20u ? pb + e1 * 8 : trash;
      pa2 = (unsigned)(e1 + 16) < 20u ? pb + (e1 + 16) * 8 : trash; pa3 = l15 == 0 ? pb + 160 : trash;
      PINV(pa0); PINV(pa1); PINV(pa2); PINV(pa3);
    };
    if (wv >= 4) {
      int ks = k0 == 9 ? 0 : k0 + 1;
      issue_x(ks, L_KVSLOT); waitv<4>(); rawbar();
      ST(14);
      issue_kw(ks, L_KVSLOT);
      unsigned so = 2 * L_KVSLOT;
#pragma unroll 1
      for (int s = 1; s < 9; ++s) { ks = ks == 9 ? 0 : ks + 1; issue_x(ks, so); issue_kw(ks, so); so = (so == 3 * L_KVSLOT) ? 0 : so + L_KVSLOT; waitv<9>(); rawbar(); }
      ST(15);
      part_addr();
      if (wv < 7) {
        v4u hd[6]; v2u hm[6];
#pragma unroll
        for (int i = 0; i < 6; ++i) { const int u = lw + 3 * i; const unsigned o = (80 * g + 4 * rt) * 4096 + u * 1024 + lane * 16;
          hd[i] = __builtin_amdgcn_raw_buffer_load_b128(bh, u < 16 ? o : OOB, 0, 0);
          hm[i] = __builtin_amdgcn_raw_buffer_load_b64(bh, (u >= 16 || ((u & 3) == 0 && lane == 0)) ? OOB : o - 8, 0, 0); }
        SCHED();
        GET_TAG();
        waitv<12>(); bar();
        h_pads(L_KVCONV + lw * L_CH);
        bar();
        PINV(hd[5]); PINV(hm[5]);
#pragma unroll
        for (int i = 0; i < 6; ++i) { const int u = lw + 3 * i; if (u < 16) h_store(L_KVCONV + (u >> 2) * L_CH, u & 3, hd[i], hm[i]); }
        if (wv > 4) { const unsigned wov = lane < 50 ? (iq * 400 + (wv - 5) * 200 + lane) * 128 : OOB;
          warm(ro, wov, 0); warm(ro, wov, 6400); warm(ro, wov, 12800); warm(ro, wov, 19200); }
      } else {
        GET_TAG();
        waitv<0>(); bar();
        h_pads(L_KVCONV + lw * L_CH);
        bar();
        wait_flags(p.ws, FL_U + (g >> 1) * 32 + (lane & 31), tag);
#pragma unroll 1
        for (int c = 0; c < 4; ++c) load_u(c0 - 3 + c, L_KVCONV + c * L_CH);
      }
      ST(1);
    } else {
      if (tid == 0) { asm volatile("" :: "v"(smem)); sts32(L_CNT + 4, 0); }
      GET_TAG();
      part_addr();
      const int hf = wv & 1;
      v4f acc[4][5];
#pragma unroll
      for (int m = 0; m < 4; ++m)
#pragma unroll
        for (int n = 0; n < 5; ++n) { const float c = (n < 2 || (n == 2 && !hf)) ? 1e-6f : 0.f; acc[m][n] = (v4f){c, c, c, c}; }
      const unsigned xa = ((wv >> 1) * 64 + l15) * 128 + ((grp ^ (l15 >> 1)) << 4);
      gemm_kv(acc, xa, 16384 + trl + hf * 96, 16384 + trl + 64 + hf * 10240 STPASS);
      const unsigned fa = (wv >> 1) * 2048 + lane * 16 + hf * 3 * 4096;
#pragma unroll
      for (int t = 0; t < 2; ++t) {
#pragma unroll
        for (int n = 0; n < 2; ++n) sts128(fa + n * 4096 + t * 1024, cat(phi4(acc[2 * t][n]), phi4(acc[2 * t + 1][n])));
#pragma unroll
        for (int n = 3; n < 5; ++n) sts128(fa + (n + 2) * 4096 + t * 1024, cat(pk4(acc[2 * t][n]), pk4(acc[2 * t + 1][n])));
      }
      if (hf) {
#pragma unroll
        for (int t = 0; t < 2; ++t) sts128(fa + 4 * 4096 + t * 1024, cat(pk4(acc[2 * t][2]), pk4(acc[2 * t + 1][2])));
      } else {
#pragma unroll
        for (int t = 0; t < 2; ++t) sts128(fa + 2 * 4096 + t * 1024, cat(phi4(acc[2 * t][2]), phi4(acc[2 * t + 1][2])));
      }
    }
    bar();
    ST(13);
    if (wv < 5) {
      const unsigned ka = wv * 4096 + lane * 16;
      v4u pk[4], v0[4], v1[4], v2[4];
#pragma unroll
      for (int t = 0; t < 4; ++t) { pk[t] = lds128(ka + t * 1024); v1[t] = lds128(ka + 5 * 4096 + t * 1024); }
#pragma unroll
      for (int t = 0; t < 4; ++t) { v0[t] = lds128(ka + 4 * 4096 + t * 1024); v2[t] = lds128(ka + 6 * 4096 + t * 1024); }
      SCHED();
      const v4u ones = {0x3f803f80u, 0x3f803f80u, 0x3f803f80u, 0x3f803f80u};
      v4f d0 = {0, 0, 0, 0}, d1 = d0, d2 = d0, dz = d0;
#pragma unroll
      for (int t = 0; t < 4; ++t) { dz = mfma(pk[t], ones, dz); d1 = mfma(pk[t], v1[t], d1); }
      if (wv > 0) {
#pragma unroll
        for (int t = 0; t < 4; ++t) d0 = mfma(pk[t], v0[t], d0);
      }
      if (wv < 4) {
#pragma unroll
        for (int t = 0; t < 4; ++t) d2 = mfma(pk[t], v2[t], d2);
      }
      sts64(pa3, pk4(dz)); sts64(pa1, pk4(d1)); sts64(pa0, pk4(d0)); sts64(pa2, pk4(d2));
      ST(2);
    } else if (wv == 7) waitv<0>();
    bar();
    ST(4);
    if (wv < 4) {
      conv_wave<255>(L_KVCONV + wv * L_CH, dbits, p.ws, WS_YT + c0 * 4096, lane STPASS);
      ST(5);
      arrive_flag(L_CNT + 4, 4, p.ws, FL_Y + g * 32 + rt, tag, lane);
    } else {
      if (wv == 4) {
#pragma unroll
        for (int j = 0; j < 4; ++j)
          __builtin_amdgcn_raw_buffer_store_b128(lds128(L_PC + j * 1024 + lane * 16), bws, (j < 3 || lane < 18) ? j * 1024 + lane * 16 : OOB, WS_PART + (g * 16 + rt) * PART_STRIDE, 16);
        waitv<0>();
        if (lane == 0) set_flag(p.ws, FL_P + g * 16 + rt, tag);
        ST(16);
        load_q(0, 1);
      } else { __builtin_amdgcn_s_sleep(4); load_wo(wv - 5, 3, 0, 12); }
      bar();
      load_wo(lw, 4, 12, 20); waitv<0>();
    }
    bar();
  } else {
    auto prepass = [&](int wu) {
      const int tg = lane & 7, cp = 8 * wu + (lane >> 3), c = 160 * cq + 2 * cp;
      const v4u sw = lds128(L_SW + cp * 16);
      unsigned ka = L_QKIMG + tg * 2576 + cp * 4; PINV(ka);
      const unsigned va = ka + (L_QVIMG - L_QKIMG);
      v2f kk[11];
#pragma unroll
      for (int j = 0; j < 11; ++j) { const unsigned u = lds32(ka + (j < 3 ? (5 + j) * 320 : 2576 + (j - 3) * 320)); kk[j] = (v2f){blo(u), bhi(u)}; }
      const v2f w0 = {blo(sw.x), blo(sw.z)}, w1 = {bhi(sw.x), bhi(sw.z)}, w2 = {blo(sw.y), blo(sw.w)}, w3 = {bhi(sw.y), bhi(sw.w)};
      v2f uu[8];
#pragma unroll
      for (int j = 0; j < 8; ++j) { const unsigned u = lds32(va + j * 320);
        uu[j] = (kk[j] * w3 + kk[j + 1] * w2 + kk[j + 2] * w1 + kk[j + 3] * w0) * (v2f){blo(u), bhi(u)}; }
      const unsigned uo = WS_UT + c * 4096 + 2 * (2040 - t0 - 8 * tg);
      __builtin_amdgcn_raw_buffer_store_b128((v4u){pk2(uu[7].x, uu[6].x), pk2(uu[5].x, uu[4].x), pk2(uu[3].x, uu[2].x), pk2(uu[1].x, uu[0].x)}, bws, uo, 0, 16);
      __builtin_amdgcn_raw_buffer_store_b128((v4u){pk2(uu[7].y, uu[6].y), pk2(uu[5].y, uu[4].y), pk2(uu[3].y, uu[2].y), pk2(uu[1].y, uu[0].y)}, bws, uo + 4096, 0, 16);
    };
    if (tid == 0) { asm volatile("" :: "v"(smem)); sts32(L_CNT, 0); sts32(L_CNT + 4, 0); }
    GET_TAG();
    if (wv < 4) {
      waitv<0>(); bar();
      ST(1);
#pragma unroll 1
      for (int wu = wv; wu < 10; wu += 4) prepass(wu);
      ST(2);
      arrive_flag(L_CNT + 4, 4, p.ws, FL_U + cq * 32 + tt, tag, lane);
      ST(13);
      v4f acc[2][5];
#pragma unroll
      for (int m = 0; m < 2; ++m)
#pragma unroll
        for (int n = 0; n < 5; ++n) acc[m][n] = (v4f){0, 0, 0, 0};
      const unsigned xa = (wv * 32 + l15) * 128 + ((grp ^ (l15 >> 1)) << 4);
      gemm_q(acc, xa, 16384 + trl);
      ST(3);
#pragma unroll
      for (int n = 0; n < 5; ++n) { pq[n] = pk4(acc[0][n]); sts64(L_QX + wv * 2560 + n * 512 + lane * 8, pk4(acc[1][n])); }
      bar();
    } else {
      if (lw == 2) warm_x(2); else warm_w(2 + lw - (lw >> 1));
      waitv<1>(); bar();
      loader_offsets();
      waitv<0>();
      if (lw & 1) warm_w(5 + (lw >> 1)); else warm_x(3 + (lw >> 1));
      { const unsigned r5 = lane / 5, rho = 10 * lw + r5; int kb = k0 + (rho >> 2); kb = kb >= 10 ? kb - 10 : kb;
        unsigned vo = lane < 50 ? (kb * 64 + iq * 4 + (rho & 3)) * 3840 + (xcd & 1) * 640 + (lane - r5 * 5) * 128 : OOB; PINV(vo);
        waitv<0>(); ST(14); warm(rw, vo, 0); }
      const unsigned wcol = 160 * g + (lw >> 1) * 32 * 3840, wl = 16384 + (lw >> 1) * 5120;
      const unsigned w0 = (lw & 1) ? wo_[1] : wo_[0], w1 = (lw & 1) ? wo_[3] : wo_[2];
      int ks = k0;
      unsigned so = 0;
#pragma unroll 1
      for (int s = -1; s < 9; ++s) {
        issue_x(ks, so);
        dma(rw, w0, wcol + ks * 64 * 3840, so + wl + (lw & 1) * 1024);
        dma(rw, w1, wcol + ks * 64 * 3840, so + wl + (lw & 1) * 1024 + 2048);
        if (lw & 1) waitv<6>(); else { dma(rw, wo_[4], wcol + ks * 64 * 3840, so + wl + 4096); waitv<7>(); }
        ks = ks == 9 ? 0 : ks + 1; so = (so == 3 * L_QSLOT) ? 0 : so + L_QSLOT;
        if (s >= 0) rawbar();
        if (s == 0) ST(13);
        if (s == 1) ST(2);
        if (s == 4) ST(3);
      }
      ST(16);
      const v4u hd = __builtin_amdgcn_raw_buffer_load_b128(bh, c0 * 4096 + lw * 1024 + lane * 16, 0, 0);
      const v2u hm = __builtin_amdgcn_raw_buffer_load_b64(bh, (lw == 0 && lane == 0) ? OOB : (unsigned)(c0 * 4096 + lw * 1024 + lane * 16 - 8), 0, 0);
      SCHED();
      waitv<2>(); bar();
      if (wv == 7) { wait_flags(p.ws, FL_U + (g >> 1) * 32 + (lane & 31), tag); load_u(c0, L_QCONV); }
      if (wv == 4) h_pads(L_QCONV);
      ST(17);
      bar();
      h_store(L_QCONV, lw, hd, hm);
      waitv<0>();
      bar();
#pragma unroll
      for (int n = 0; n < 5; ++n) pq[n] = lds64(L_QX + lw * 2560 + n * 512 + lane * 8);
    }
#pragma unroll
    for (int n = 0; n < 5; ++n) { pq[n] = (v2u){phi2(pq[n].x), phi2(pq[n].y)}; PINV(pq[n]); }
    ST(4);
    if (wv < 4) {
      const unsigned yo = WS_YT + c0 * 4096;
      if (wv == 0) conv_wave<0x81>(L_QCONV, dbits, p.ws, yo, lane STPASS);
      else if (wv == 1) conv_wave<0x42>(L_QCONV, dbits, p.ws, yo, lane STPASS);
      else if (wv == 2) conv_wave<0x24>(L_QCONV, dbits, p.ws, yo, lane STPASS);
      else conv_wave<0x18>(L_QCONV, dbits, p.ws, yo, lane STPASS);
      ST(5);
    } else if (wv < 7) { load_wo(lw, 3, 0, 20); load_q(lw, 3); }
    unsigned ka[13], za[5];
    {
      int n = 0;
#pragma unroll
      for (int a = 0; a < 5; ++a) {
        const int Dq = 4 * a + grp, hh = Dq / 5, eb = l15 - 20 * hh;
        za[a] = L_KVC + ((hh == (l15 & 3)) ? Dq * 21 + 20 : 420) * 8; PINV(za[a]);
#pragma unroll
        for (int bb = 0; bb < 5; ++bb) if (bb >= a - 1 && bb <= a + 1) { const int e = 16 * bb + eb; ka[n] = L_KVC + (((unsigned)e < 20u) ? Dq * 21 + e : 420) * 8; PINV(ka[n]); ++n; }
      }
    }
    unsigned po = (tid < 210) ? WS_PART + g * 16 * PART_STRIDE + tid * 16 : OOB; PINV(po);
    if (wv < 4) arrive_flag(L_CNT, 4, p.ws, FL_Y + g * 32 + 16 + rt, tag, lane);
    else if (wv == 7) wait_flags(p.ws, FL_P + g * 16 + l15, tag);
    bar();
    ST(6);
    if (wv < 4) {
      v4u r[16];
#pragma unroll
      for (int j = 0; j < 16; ++j) r[j] = __builtin_amdgcn_raw_buffer_load_b128(bws, po, j * PART_STRIDE, 16);
      v4f s0 = {0, 0, 0, 0}, s1 = s0;
      unsigned sel0 = 0x00003f80u, sel1 = 0x3f800000u; PINV(sel0); PINV(sel1);
      auto dot = [](unsigned x, unsigned sel, float c) { return __builtin_amdgcn_fdot2_f32_bf16(__builtin_bit_cast(v2bf, x), __builtin_bit_cast(v2bf, sel), c, false); };
#pragma unroll
      for (int j = 0; j < 16; ++j) { s0[0] = dot(r[j].x, sel0, s0[0]); s0[1] = dot(r[j].x, sel1, s0[1]); s0[2] = dot(r[j].y, sel0, s0[2]); s0[3] = dot(r[j].y, sel1, s0[3]);
        s1[0] = dot(r[j].z, sel0, s1[0]); s1[1] = dot(r[j].z, sel1, s1[1]); s1[2] = dot(r[j].w, sel0, s1[2]); s1[3] = dot(r[j].w, sel1, s1[3]); }
      sts128(L_KVC + tid * 16, cat(pk4(s0), pk4(s1)));
    }
    waitv<0>(); bar();
    ST(7);
    {
      const v2u z2 = {0, 0};
      const v4u s0 = cat(pq[0], pq[1]), s1 = cat(pq[2], pq[3]), s2 = cat(pq[4], z2);
      const v4f zero = {0, 0, 0, 0};
      v4f den = mfma(cat(lds64(za[0]), lds64(za[1])), s0, zero); den = mfma(cat(lds64(za[2]), lds64(za[3])), s1, den); den = mfma(cat(lds64(za[4]), z2), s2, den);
      v4f n0 = mfma(cat(lds64(ka[0]), lds64(ka[2])), s0, zero);
      v4f n1 = mfma(cat(lds64(ka[1]), lds64(ka[3])), s0, zero); n1 = mfma(cat(lds64(ka[5]), z2), s1, n1);
      v4f n2 = mfma(cat(z2, lds64(ka[4])), s0, zero); n2 = mfma(cat(lds64(ka[6]), lds64(ka[8])), s1, n2);
      v4f n3 = mfma(cat(lds64(ka[7]), lds64(ka[9])), s1, zero); n3 = mfma(cat(lds64(ka[11]), z2), s2, n3);
      v4f n4 = mfma(cat(z2, lds64(ka[10])), s1, zero); n4 = mfma(cat(lds64(ka[12]), z2), s2, n4);
      const float r0 = __builtin_amdgcn_rcpf(den[0] + 1e-6f), r1 = __builtin_amdgcn_rcpf(den[1] + 1e-6f), r2 = __builtin_amdgcn_rcpf(den[2] + 1e-6f), r3 = __builtin_amdgcn_rcpf(den[3] + 1e-6f);
      n0 *= r0; n1 *= (grp < 1) ? r0 : r1; n2 *= (grp < 2) ? r1 : r2; n3 *= (grp < 3) ? r2 : r3; n4 *= r3;
      const int mt = rt * 8 + 2 * lw + (wv >> 2);
      const unsigned oo = WS_OF + (mt * 8 + g) * 2560;
      __builtin_amdgcn_raw_buffer_store_b128(cat(pk4(n0), pk4(n1)), bws, lane * 16, oo, 16);
      __builtin_amdgcn_raw_buffer_store_b128(cat(pk4(n2), pk4(n3)), bws, lane * 16 + 1024, oo, 16);
      __builtin_amdgcn_raw_buffer_store_b64(pk4(n4), bws, lane * 8 + 2048, oo, 16);
      ST(8);
      waitv<0>();
      if (lane == 0) set_flag(p.ws, FL_O + mt * 8 + g, tag);
    }
  }
  ST(9);
#define AUX_B 0
#define AUX_A 0
  const __amdgpu_buffer_rsrc_t bout = __builtin_amdgcn_make_buffer_rsrc(p.out, 0, 2048 * 2560, 0x00020000);
  if (wv < 4) {
    const int mt = (gr0 >> 4) + wv;
    unsigned wb0 = L_WO + trl, wb1 = wb0 + 51200; PINV(wb0); PINV(wb1);
    v4u wf[7][5];
#define WRD(t, n) do { const int y_ = (t) / 5, s_ = (t) % 5; const unsigned ra = (s_ == 4) ? 64 : (s_ >> 1) * 80 + (s_ & 1) * 32, rb = (s_ == 4) ? 144 : ra + 16, wb = (y_ & 2) ? wb1 : wb0; \
      wf[(t) % 7][n] = cat(ldtr(wb + (y_ & 1) * 25600 + ra * 160 + (n) * 32), ldtr(wb + (y_ & 1) * 25600 + rb * 160 + (n) * 32)); } while (0)
#pragma unroll
    for (int f = 0; f < 35; ++f) WRD(f / 5, f % 5);
    unsigned ob = (gr0 + wv * 16 + l15) * 2560 + 1280 + gct * 160 + grp * 8; PINV(ob);
    SCHED();
    wait_flags(p.ws, FL_O + mt * 8 + (lane & 7), tag);
    ST(10);
    v4u of[20];
#pragma unroll
    for (int y = 0; y < 4; ++y) {
      const unsigned oo = WS_OF + (mt * 8 + 2 * y) * 2560;
      of[5 * y + 0] = __builtin_amdgcn_raw_buffer_load_b128(bws, lane * 16, oo, 16);
      of[5 * y + 1] = __builtin_amdgcn_raw_buffer_load_b128(bws, lane * 16 + 1024, oo, 16);
      of[5 * y + 2] = __builtin_amdgcn_raw_buffer_load_b128(bws, lane * 16 + 2560, oo, 16);
      of[5 * y + 3] = __builtin_amdgcn_raw_buffer_load_b128(bws, lane * 16 + 3584, oo, 16);
      of[5 * y + 4] = cat(__builtin_amdgcn_raw_buffer_load_b64(bws, lane * 8 + 2048, oo, 16), __builtin_amdgcn_raw_buffer_load_b64(bws, lane * 8 + 2048, oo + 2560, 16));
    }
    SCHED();
    v4f acc[5];
#pragma unroll
    for (int n = 0; n < 5; ++n) acc[n] = (v4f){0, 0, 0, 0};
#pragma unroll
    for (int t = 0; t < 20; ++t) {
      v4u cur[5];
#pragma unroll
      for (int n = 0; n < 5; ++n) cur[n] = wf[t % 7][n];
#pragma unroll
      for (int f = 0; f < 65; ++f) if (t >= 1 && f >= (t - 1) * 15 / 4 && f < t * 15 / 4) WRD(7 + f / 5, f % 5);
#pragma unroll
      for (int n = 0; n < 5; ++n) acc[n] = mfma(cur[n], of[t], acc[n]);
      __builtin_amdgcn_sched_group_barrier(0x8, 1, 0); __builtin_amdgcn_sched_group_barrier(0x100, 2, 0);
      __builtin_amdgcn_sched_group_barrier(0x8, 1, 0); __builtin_amdgcn_sched_group_barrier(0x100, 1, 0);
      __builtin_amdgcn_sched_group_barrier(0x8, 1, 0); __builtin_amdgcn_sched_group_barrier(0x100, 2, 0);
      __builtin_amdgcn_sched_group_barrier(0x8, 1, 0); __builtin_amdgcn_sched_group_barrier(0x100, 1, 0);
      __builtin_amdgcn_sched_group_barrier(0x8, 1, 0); __builtin_amdgcn_sched_group_barrier(0x100, 2, 0);
      SCHED();
    }
#undef WRD
#pragma unroll
    for (int n = 0; n < 5; ++n) __builtin_amdgcn_raw_buffer_store_b64(pk4(acc[n]), bout, ob + n * 32, 0, AUX_B);
    ST(11);
    if (b == 0 && wv == 0) {
      wait_flags(p.ws, FL_U + lane, tag); wait_flags(p.ws, FL_U + 64 + lane, tag);
      if (lane == 0) __hip_atomic_store((GAS u64*)p.ws, tag, RLX_AGENT);
    }
  } else {
    const int tg = lane & 7;
    unsigned yo = WS_YT + (80 * gct + 16 * lw + 2 * (lane >> 3)) * 4096 + 2 * (gr0 + 8 * tg); PINV(yo);
    wait_flags(p.ws, FL_Y + gct * 32 + (lane & 31), tag);
    ST(10);
    v4u y0 = __builtin_amdgcn_raw_buffer_load_b128(bws, yo, 0, 16), y1 = __builtin_amdgcn_raw_buffer_load_b128(bws, yo, 4096, 16);
    v4u y2 = y0, y3 = y1;
    if (lw == 0) { y2 = __builtin_amdgcn_raw_buffer_load_b128(bws, yo, 64 * 4096, 16); y3 = __builtin_amdgcn_raw_buffer_load_b128(bws, yo, 65 * 4096, 16); }
#pragma unroll 1
    for (int cb = lw; cb < 5; cb += 4) {
      const unsigned qa = L_QT + tg * 1296 + (8 * cb + (lane >> 3)) * 4;
#pragma unroll
      for (int j = 0; j < 8; ++j) { const unsigned u = lds32(qa + j * 160);
        const float a0 = (j & 1) ? bhi(y0[j >> 1]) : blo(y0[j >> 1]), a1 = (j & 1) ? bhi(y1[j >> 1]) : blo(y1[j >> 1]);
        sts32(qa + j * 160, pk2(blo(u) * a0, bhi(u) * a1)); }
      asm volatile("s_waitcnt lgkmcnt(0)" ::: "memory");
#pragma unroll
      for (int j = 0; j < 2; ++j) { const int row = j * 32 + (lane >> 1);
        const v4u o = lds128(L_QT + (row >> 3) * 1296 + (row & 7) * 160 + cb * 32 + (lane & 1) * 16);
        __builtin_amdgcn_raw_buffer_store_b128(o, bout, (gr0 + row) * 2560 + gct * 160 + cb * 32 + (lane & 1) * 16, 0, AUX_A); }
      y0 = y2; y1 = y3;
    }
    ST(11);
  }
}

extern "C" void kernel_launch(void* const* d_in, const int* in_sizes, int n_in, void* d_out, int out_size, void* d_ws, size_t ws_size, hipStream_t stream) {
  Params p;
  p.q = (const u16*)d_in[0]; p.k = (const u16*)d_in[1]; p.v = (const u16*)d_in[2]; p.sk = (const u16*)d_in[3]; p.h = (const u16*)d_in[4];
  p.Dv = (const u16*)d_in[5]; p.x = (const u16*)d_in[6]; p.w = (const u16*)d_in[7]; p.wo = (const u16*)d_in[8];
  p.out = (u16*)d_out; p.ws = (char*)d_ws;
  fused_kernel<<<256, 512, 0, stream>>>(p);
}
```

```cpp
#include <hip/hip_runtime.h>
typedef unsigned short u16;
typedef unsigned long long u64;
typedef unsigned v2u __attribute__((ext_vector_type(2)));
typedef unsigned v4u __attribute__((ext_vector_type(4)));
typedef int v4i __attribute__((ext_vector_type(4)));
typedef float v2f __attribute__((ext_vector_type(2)));
typedef float v4f __attribute__((ext_vector_type(4)));
typedef short v4s __attribute__((ext_vector_type(4)));
typedef __bf16 v2bf __attribute__((ext_vector_type(2)));
typedef __bf16 v8bf __attribute__((ext_vector_type(8)));
#define GAS __attribute__((address_space(1)))
#define LAS __attribute__((address_space(3)))
#define RLX_AGENT __ATOMIC_RELAXED, __HIP_MEMORY_SCOPE_AGENT
#define DEVINL __device__ __forceinline__

constexpr unsigned WS_FLAG = 4096;
constexpr unsigned FL_U = 0, FL_P = 128, FL_Y = 256, FL_O = 512;
constexpr unsigned FL_N = 1024;
constexpr unsigned WS_UT = 2u << 20, WS_YT = 5u << 20, WS_PART = 8u << 20, WS_OF = 9u << 20, WS_STAMP = 12u << 20;
constexpr unsigned WS_BYTES = 13u << 20;
constexpr unsigned PART_STRIDE = 3584;
constexpr unsigned OOB = 0x80000000u;
constexpr unsigned L_KVSLOT = 36992, L_WV = 16384 + 10256, L_QSLOT = 26624;
constexpr unsigned L_CS = 4160, L_UOFF = 4 * L_CS, L_CH = 21248;
constexpr unsigned L_KVCONV = 73984, L_QKIMG = 79872, L_QVIMG = 79872 + 9 * 2576, L_CONV5 = 73984 - 21248, L_RED = 0;
constexpr unsigned L_YT = 113664, L_ST = 146432, L_KVC = 156672, L_PC = 158976, L_DUMMY = 162816, L_CNT = 163584;
constexpr unsigned L_QT = 0, L_WO = 11264, L_SW = 161280;
#define CODE_BYTES 40000

struct Params { const u16 *q, *k, *v, *sk, *h, *Dv, *x, *w, *wo; u16* out; char* ws; };

DEVINL v4i mk_rsrc(const void* p, unsigned bytes) {
  u64 a = (u64)p; v4i r; r.x = (int)(unsigned)a; r.y = (int)((unsigned)(a >> 32) & 0xffffu); r.z = (int)bytes; r.w = 0x00020000; return r;
}
DEVINL void dma(v4i rs, unsigned voff, unsigned soff, unsigned lds) {
  asm volatile("s_mov_b32 m0, %3\n\ts_nop 0\n\tbuffer_load_dwordx4 %0, %1, %2 offen lds" :: "v"(voff), "s"(rs), "s"(soff), "s"(lds) : "memory");
}
DEVINL void warm_sc1(v4i rs, unsigned voff, unsigned soff) {
  asm volatile("s_mov_b32 m0, %3\n\ts_nop 0\n\tbuffer_load_dword %0, %1, %2 offen sc1 lds" :: "v"(voff), "s"(rs), "s"(soff), "s"(L_DUMMY) : "memory");
}
DEVINL void dma_sc1(v4i rs, unsigned voff, unsigned soff, unsigned lds) {
  asm volatile("s_mov_b32 m0, %3\n\ts_nop 0\n\tbuffer_load_dwordx4 %0, %1, %2 offen sc1 lds" :: "v"(voff), "s"(rs), "s"(soff), "s"(lds) : "memory");
}
DEVINL void warm(v4i rs, unsigned voff, unsigned soff) {
  asm volatile("s_mov_b32 m0, %3\n\ts_nop 0\n\tbuffer_load_dword %0, %1, %2 offen lds" :: "v"(voff), "s"(rs), "s"(soff), "s"(L_DUMMY) : "memory");
}
template <int N> DEVINL void waitv() { asm volatile("s_waitcnt vmcnt(%0)" :: "n"(N) : "memory"); }
DEVINL void rawbar() { asm volatile("s_barrier" ::: "memory"); }
DEVINL void bar() { asm volatile("s_waitcnt lgkmcnt(0)\n\ts_barrier" ::: "memory"); }
#define SCHED() __builtin_amdgcn_sched_barrier(0)
DEVINL v4u lds128(unsigned a) { return *(const LAS v4u*)(size_t)a; }
DEVINL v2u lds64(unsigned a) { return *(const LAS v2u*)(size_t)a; }
DEVINL unsigned lds32(unsigned a) { return *(const LAS unsigned*)(size_t)a; }
DEVINL void sts128(unsigned a, v4u v) { *(LAS v4u*)(size_t)a = v; }
DEVINL void sts64(unsigned a, v2u v) { *(LAS v2u*)(size_t)a = v; }
DEVINL void sts32(unsigned a, unsigned v) { *(LAS unsigned*)(size_t)a = v; }
DEVINL v2u ldtr(unsigned a) { return __builtin_bit_cast(v2u, __builtin_amdgcn_ds_read_tr16_b64_v4i16((LAS v4s*)(size_t)a)); }
DEVINL v4u cat(v2u a, v2u b) { return (v4u){a.x, a.y, b.x, b.y}; }
DEVINL v4f mfma(v4u a, v4u b, v4f c) { return __builtin_amdgcn_mfma_f32_16x16x32_bf16(__builtin_bit_cast(v8bf, a), __builtin_bit_cast(v8bf, b), c, 0, 0, 0); }
DEVINL unsigned pk2(float a, float b) { v2f p = {a, b}; return __builtin_bit_cast(unsigned, __builtin_convertvector(p, v2bf)); }
DEVINL v2u pk4(v4f a) { return (v2u){pk2(a[0], a[1]), pk2(a[2], a[3])}; }
DEVINL float blo(unsigned u) { return __uint_as_float(u << 16); }
DEVINL float bhi(unsigned u) { return __uint_as_float(u & 0xffff0000u); }
DEVINL unsigned phi2(unsigned u) {
  const v2f t = {blo(u), bhi(u)};
  const v2f a = t * 1.44269504089f, c = t + 1e-6f;
  const v2f e = {__builtin_amdgcn_fmed3f(__builtin_amdgcn_exp2f(a.x), 0.f, 1.f), __builtin_amdgcn_fmed3f(__builtin_amdgcn_exp2f(a.y), 0.f, 1.f)};
  const v2f m = {fmaxf(c.x, 1e-6f), fmaxf(c.y, 1e-6f)};
  const v2f r = m + e;
  return pk2(r.x, r.y);
}
DEVINL v2u phi4(v4f c) {
  const v4f a = c * 1.44269504089f - 1.44269504089e-6f;
  v4f r;
#pragma unroll
  for (int j = 0; j < 4; ++j) r[j] = __int_as_float(max(__float_as_int(c[j]), 0x358637bd));
  const v4f e = {__builtin_amdgcn_fmed3f(__builtin_amdgcn_exp2f(a[0]), 0.f, 1.f), __builtin_amdgcn_fmed3f(__builtin_amdgcn_exp2f(a[1]), 0.f, 1.f),
                 __builtin_amdgcn_fmed3f(__builtin_amdgcn_exp2f(a[2]), 0.f, 1.f), __builtin_amdgcn_fmed3f(__builtin_amdgcn_exp2f(a[3]), 0.f, 1.f)};
  return pk4(r + e);
}
DEVINL float add1(float a, float b) { float r; asm("v_add_f32 %0, %1, %2" : "=v"(r) : "v"(a), "v"(b)); return r; }
DEVINL float mul1(float a, float b) { float r; asm("v_mul_f32 %0, %1, %2" : "=v"(r) : "v"(a), "v"(b)); return r; }
DEVINL v4f add4(v4f a, v4f b) { return (v4f){add1(a[0], b[0]), add1(a[1], b[1]), add1(a[2], b[2]), add1(a[3], b[3])}; }
DEVINL v4f mul4(v4f a, v4f b) { return (v4f){mul1(a[0], b[0]), mul1(a[1], b[1]), mul1(a[2], b[2]), mul1(a[3], b[3])}; }
#define PINV(x) asm volatile("" : "+v"(x))
#define PINS(x) asm volatile("" : "+s"(x))

DEVINL void set_flag(char* ws, unsigned idx, u64 tag, int nrep, int lane) {
  const __amdgpu_buffer_rsrc_t rs = __builtin_amdgcn_make_buffer_rsrc(ws, 0, WS_BYTES, 0x00020000);
  const unsigned lo = (unsigned)tag, hi = (unsigned)(tag >> 32);
  if (lane < 8 * nrep) __builtin_amdgcn_raw_buffer_store_b128((v4u){lo, hi, lo, hi}, rs, (lane >> 3) * (FL_N * 128) + (lane & 7) * 16, WS_FLAG + idx * 128, 16);
}
#define WO_GATE 16
#define NREP 8
#define HSPLIT 2
#define RED_GATE 8
#define PUBSLEEP 1
#define PSLEEP 2
DEVINL void wait_flags(char* ws, unsigned idx, int n, u64 tag, int lane) {
  if (lane < n) {
    GAS u64* p = (GAS u64*)(ws + WS_FLAG + (idx + lane) * 128u);
#pragma unroll 1
    for (unsigned spins = 0; spins < (1u << 16); ++spins) {
      if (__hip_atomic_load(p, RLX_AGENT) == tag) break;
      __builtin_amdgcn_s_sleep(PSLEEP);
    }
  }
  __builtin_amdgcn_fence(__ATOMIC_ACQUIRE, "wavefront");
  asm volatile("" ::: "memory");
}
DEVINL void arrive_flag(unsigned cnt, unsigned n, char* ws, unsigned idx, u64 tag, int lane) {
  unsigned old;
  asm volatile("s_waitcnt vmcnt(0)\n\ts_mov_b64 exec, 1\n\tds_add_rtn_u32 %0, %1, %2\n\ts_mov_b64 exec, -1\n\ts_waitcnt lgkmcnt(0)" : "=&v"(old) : "v"(cnt), "v"(1u) : "memory");
  if (__builtin_amdgcn_readfirstlane(old) == n - 1) set_flag(ws, idx, tag, NREP, lane);
}
DEVINL void lds_arrive(unsigned cnt, int lane) {
  asm volatile("s_mov_b64 exec, 1\n\tds_add_u32 %0, %1\n\ts_mov_b64 exec, -1" :: "v"(cnt), "v"(1u) : "memory");
}
template <int SLEEP> DEVINL void lds_wait(unsigned cnt, unsigned n) {
  unsigned v, sv, spins;
  asm volatile("s_mov_b32 %2, 0x100000\n1:\n\tds_read_b32 %0, %3\n\ts_waitcnt lgkmcnt(0)\n\tv_readfirstlane_b32 %1, %0\n\ts_cmp_ge_u32 %1, %4\n\ts_cbranch_scc1 2f\n\t"
               "s_sleep %5\n\ts_sub_u32 %2, %2, 1\n\ts_cbranch_scc0 1b\n2:"
               : "=&v"(v), "=&s"(sv), "=&s"(spins) : "v"(cnt), "s"(n), "n"(SLEEP) : "memory", "scc");
}
#define ST(i)
#define STC(i)
#define STARG
#define STPASS

DEVINL void gemm_kv(v4f (&aK)[8], v4f (&aV)[8], v4f (&aT)[4], unsigned xa, unsigned xb, unsigned wa, unsigned wt STARG) {
  v4u xp[8], wp[3], xq[8], wq[3];
  unsigned so = L_KVSLOT;
#define RD(X, W, hs) do { X[0] = lds128((xa + so) ^ ((hs) * 64)); \
    W[0] = cat(ldtr(wa + so + (hs) * 5120), ldtr(wa + so + (hs) * 5120 + 2560)); \
    W[1] = cat(ldtr(wa + so + (hs) * 5120 + 10256), ldtr(wa + so + (hs) * 5120 + 2560 + 10256)); \
    W[2] = cat(ldtr(wt + so + (hs) * 5120), ldtr(wt + so + (hs) * 5120 + 2560)); \
    _Pragma("unroll") for (int m = 1; m < 8; ++m) X[m] = lds128((((m < 4 ? xa : xb) + so) ^ ((hs) * 64)) + (m & 3) * 2048); } while (0)
#define MM(X, W) do { _Pragma("unroll") for (int m = 0; m < 8; ++m) { aK[m] = mfma(X[m], W[0], aK[m]); aV[m] = mfma(X[m], W[1], aV[m]); if (m < 4) aT[m] = mfma(X[m], W[2], aT[m]); } } while (0)
#define MIX() do { _Pragma("unroll") for (int i = 0; i < 20; ++i) { __builtin_amdgcn_sched_group_barrier(0x8, 1, 0); __builtin_amdgcn_sched_group_barrier(0x100, 1, 0); } } while (0)
  bar(); SCHED();
  RD(xp, wp, 0);
  SCHED();
#pragma unroll 1
  for (int s = 0; s < 9; ++s) {
    RD(xq, wq, 1); MM(xp, wp); MIX(); SCHED();
    bar(); SCHED();
    so = (so == 0) ? 3 * L_KVSLOT : so - L_KVSLOT;
    RD(xp, wp, 0); MM(xq, wq); MIX(); SCHED();
  }
  RD(xq, wq, 1); MM(xp, wp); MIX(); SCHED();
  ST(1);
#pragma unroll
  for (int m = 0; m < 4; ++m) aT[m] = mfma(xq[m], wq[2], aT[m]);
#pragma unroll
  for (int m = 0; m < 8; ++m) aK[m] = mfma(xq[m], wq[0], aK[m]);
#pragma unroll
  for (int m = 0; m < 8; ++m) aV[m] = mfma(xq[m], wq[1], aV[m]);
#undef RD
#undef MM
#undef MIX
}
DEVINL void gemm_q(v4f (&acc)[2][5], unsigned xa, unsigned wa, char* ws, unsigned uidx, u64 tag, int lane) {
  v4u xp[2][2], wp[2][5], xq[2][2], wq[2][5];
  unsigned so = 0;
#define RD(X, W) do { _Pragma("unroll") for (int hs = 0; hs < 2; ++hs) { X[hs][0] = lds128((xa + so) ^ (hs * 64)); \
    _Pragma("unroll") for (int n = 0; n < 5; ++n) W[hs][n] = cat(ldtr(wa + so + hs * 5120 + n * 32), ldtr(wa + so + hs * 5120 + 2560 + n * 32)); \
    X[hs][1] = lds128(((xa + so) ^ (hs * 64)) + 2048); } so = (so == 3 * L_QSLOT) ? 0 : so + L_QSLOT; } while (0)
#define MM(X, W) do { _Pragma("unroll") for (int hs = 0; hs < 2; ++hs) _Pragma("unroll") for (int m = 0; m < 2; ++m) _Pragma("unroll") for (int n = 0; n < 5; ++n) \
    acc[m][n] = mfma(W[hs][n], X[hs][m], acc[m][n]); } while (0)
#define MIX() do { _Pragma("unroll") for (int i = 0; i < 20; ++i) { __builtin_amdgcn_sched_group_barrier(0x8, 1, 0); __builtin_amdgcn_sched_group_barrier(0x100, 1, 0); } \
    __builtin_amdgcn_sched_group_barrier(0x100, 4, 0); } while (0)
  arrive_flag(L_CNT + 4, 4, ws, uidx, tag, lane);
  bar(); SCHED();
  RD(xp, wp);
  SCHED();
#pragma unroll 1
  for (int s = 0; s < 4; ++s) {
    rawbar(); SCHED();
    RD(xq, wq); MM(xp, wp); MIX(); SCHED();
    rawbar(); SCHED();
    RD(xp, wp); MM(xq, wq); MIX(); SCHED();
  }
  rawbar(); SCHED();
  RD(xq, wq); MM(xp, wp); MIX(); SCHED();
  lds_arrive(L_CNT + 28, lane);
  MM(xq, wq);
#undef RD
#undef MM
#undef MIX
}

struct ConvArgs { unsigned ha, ua, yo; v4u dg; };
DEVINL ConvArgs conv_args(unsigned cb, unsigned dbits, unsigned yoff, int lane) {
  const int i = lane & 15, g = lane >> 4, jt = 15 - i - 8 * g;
  ConvArgs c;
  c.ha = cb + (i >> 2) * L_CS + 16 * g + 8 * (i & 3) + 8;
  c.ua = cb + L_UOFF + 480 - 32 * i + 16 * g;
  c.yo = yoff + 16 * i + 256 * (g >> 1);
#pragma unroll
  for (int d = 0; d < 4; ++d) c.dg[d] = (jt == 2 * d) ? dbits : (jt == 2 * d + 1) ? (dbits << 16) : 0u;
  PINV(c.ha); PINV(c.ua); PINV(c.yo); PINV(c.dg);
  return c;
}
DEVINL v4u conv_pack(v4f a, v4f b) {
  v2u X = pk4(a), Y = pk4(b);
  auto r0 = __builtin_amdgcn_permlane16_swap(X.x, Y.x, false, false);
  auto r1 = __builtin_amdgcn_permlane16_swap(X.y, Y.y, false, false);
  return (v4u){r0[0], r1[0], r0[1], r1[1]};
}
DEVINL void conv_store(v4u o, int ta, unsigned yo, char* ws, int lane) {
  const __amdgpu_buffer_rsrc_t rs = __builtin_amdgcn_make_buffer_rsrc(ws, 0, WS_BYTES, 0x00020000);
  __builtin_amdgcn_raw_buffer_store_b128(o, rs, yo + ((lane & 16) ? 7 - ta : ta) * 512, 0, 16);
}
DEVINL void conv_wave(ConvArgs c5, ConvArgs c, char* ws, int wv, int lane STARG) {
  unsigned ha = c5.ha, ua = c5.ua;
  v4f acc[8], ac5[8];
  v4u F[8], G[8], F2[8], G2[8];
#define CRD(F, G) do { F[0] = cat(ldtr(ha), ldtr(ha + 8)); _Pragma("unroll") for (int v = 0; v < 8; ++v) G[v] = lds128(ua + (7 - v) * 512); \
    _Pragma("unroll") for (int v = 1; v < 8; ++v) F[v] = cat(ldtr(ha + v * 512), ldtr(ha + v * 512 + 8)); ha += 64; ua += 64; PINV(ha); PINV(ua); } while (0)
#define CMM(A, F, G) do { _Pragma("unroll") for (int w = 0; w < 8; ++w) _Pragma("unroll") for (int t = w; t < 8; ++t) A[t] = mfma(F[w], G[t - w], A[t]); } while (0)
#define CMIX() do { _Pragma("unroll") for (int j = 0; j < 36; ++j) { __builtin_amdgcn_sched_group_barrier(0x8, 1, 0); __builtin_amdgcn_sched_group_barrier(0x100, 1, 0); } } while (0)
  CRD(F, G);
  SCHED();
#pragma unroll
  for (int t = 0; t < 8; ++t) ac5[t] = mfma(c5.dg, G[t], (v4f){0, 0, 0, 0});
  SCHED();
  CRD(F2, G2); CMM(ac5, F, G); CMIX(); SCHED();
  ST(6);
  lds_wait<0>(L_CNT + 48 + wv * 4, 1);
  ST(7);
  ha = c.ha; ua = c.ua;
  CRD(F, G); CMM(ac5, F2, G2); CMIX(); SCHED();
  {
    unsigned ra = L_RED + wv * 1024 + lane * 16;
#pragma unroll
    for (int t = 0; t < 8; ++t) acc[t] = mfma(c.dg, G[t], (v4f){0, 0, 0, 0});
    CRD(F2, G2); CMM(acc, F, G);
#pragma unroll
    for (int t = 0; t < 8; ++t) sts128(ra + t * 4096, __builtin_bit_cast(v4u, ac5[t]));
#pragma unroll
    for (int j = 0; j < 24; ++j) { __builtin_amdgcn_sched_group_barrier(0x8, 1, 0); __builtin_amdgcn_sched_group_barrier(0x100, 1, 0); }
#pragma unroll
    for (int j = 0; j < 8; ++j) { __builtin_amdgcn_sched_group_barrier(0x8, 2, 0); __builtin_amdgcn_sched_group_barrier(0x200, 1, 0); }
    SCHED();
    lds_arrive(L_CNT + 12, lane);
  }
  ST(16);
#pragma unroll 1
  for (int it = 0; it < 3; ++it) {
    CRD(F, G); CMM(acc, F2, G2); CMIX(); SCHED();
    CRD(F2, G2); CMM(acc, F, G); CMIX(); SCHED();
  }
  ST(17);
  bar(); SCHED();
#undef CRD
#undef CMM
#undef CMIX
  v4u o[4];
#pragma unroll
  for (int a = 0; a < 4; ++a) {
    const int b = 7 - a;
#pragma unroll
    for (int w = 0; w <= b; ++w) { acc[b] = mfma(F2[w], G2[b - w], acc[b]); if (w <= a) acc[a] = mfma(F2[w], G2[a - w], acc[a]); }
  }
#pragma unroll
  for (int a = 0; a < 4; ++a) o[a] = conv_pack(acc[a], acc[7 - a]);
  __builtin_amdgcn_sched_group_barrier(0x8, 12, 0);
#pragma unroll
  for (int j = 0; j < 24; ++j) { __builtin_amdgcn_sched_group_barrier(0x2, 1, 0); __builtin_amdgcn_sched_group_barrier(0x8, 1, 0); }
  SCHED();
#pragma unroll
  for (int a = 0; a < 4; ++a) conv_store(o[a], a, c.yo, ws, lane);
}

__global__ void __launch_bounds__(512, 2) fused_kernel(Params p) {
  __shared__ __attribute__((aligned(1024))) char smem[163840];
  u64 pc; asm volatile("s_getpc_b64 %0" : "=s"(pc));
  const int tid = threadIdx.x, lane = tid & 63;
  const int b = blockIdx.x, xcd = b & 7, bi = b >> 3;
  warm(mk_rsrc((const void*)pc, CODE_BYTES), tid < 42 ? ((bi & 7) * 42 + tid) * 128 : OOB, 0);
  const int wv = __builtin_amdgcn_readfirstlane(tid >> 6);
  const bool isQ = bi >= 16;
  const int rt = 4 * (xcd >> 1) + (bi & 3), g = 4 * (xcd & 1) + ((bi >> 2) & 3);
  ST(0);
  typedef __attribute__((address_space(4))) const u64 cu64;
  typedef __attribute__((address_space(4))) const unsigned cu32;
  const int lw = wv & 3, iq = bi & 15;
  int k0 = ((xcd >> 1) * 5 >> 1) + 5 * (xcd & 1); k0 = k0 >= 10 ? k0 - 10 : k0;
  v4i rx, rw;
#define MK_XW() do { const u16 *x_ = p.x, *w_ = p.w; PINS(x_); PINS(w_); rx = mk_rsrc(x_, 2048 * 1280); rw = mk_rsrc(w_, 640 * 3840); } while (0)
  auto off_x = [&](int j) {
    int ka = k0 + 2 * j + (lane >> 5); ka = ka >= 10 ? ka - 10 : ka;
    unsigned vo = ((xcd >> 1) * 512 + iq * 32 + (lane & 31)) * 1280 + ka * 128; PINV(vo);
    return vo;
  };
  auto off_w = [&](int j) {
    const unsigned r10 = lane / 10, c10 = lane - r10 * 10, rho = 6 * j + r10; int kb = k0 + (rho >> 2); kb = kb >= 10 ? kb - 10 : kb;
    unsigned vo = (kb * 64 + iq * 4 + (rho & 3)) * 3840 + 1280 + (xcd & 1) * 640 + c10 * 128 + (c10 >= 5 ? 640 : 0); PINV(vo);
    vo = (lane < 60 && rho < 40) ? vo : OOB; PINV(vo);
    return vo;
  };
  auto warm_x = [&](int j) { warm(rx, off_x(j), 0); };
  auto warm_w = [&](int j) { warm(rw, off_w(j), 0); };
  unsigned xo0 = 0, xo1 = 0, wo_[5] = {0, 0, 0, 0, 0};
  auto loader_offsets = [&]() {
    const unsigned r = lane >> 3;
    xo0 = (rt * 128 + lw * 32 + r) * 1280 + (((lane & 7) ^ (r >> 1)) << 4);
    xo1 = (xo0 ^ 64) + 8 * 1280;
#pragma unroll
    for (int j = 0; j < 5; ++j) { const unsigned ch = j * 64 + lane, rho = ch / 10, c = ch - rho * 10;
      wo_[j] = (8 * ((rho >> 2) & 3) + 4 * (rho >> 4) + (rho & 3)) * 3840 + c * 16; PINV(wo_[j]); }
    PINV(xo0); PINV(xo1);
  };
  const unsigned kwcol = (640 + 640 * (lw & 1) + 80 * g) * 2 + (lw >> 1) * 32 * 3840, kwl = 16384 + (lw & 1) * 10256 + (lw >> 1) * 5120;
  auto issue_x = [&](int ks, unsigned so) {
#pragma unroll
    for (int j = 0; j < 4; ++j) dma(rx, (j & 1) ? xo1 : xo0, ks * 128 + (j >> 1) * 16 * 1280, so + lw * 4096 + j * 1024);
  };
  auto issue_kw = [&](int ks, unsigned so) {
#pragma unroll
    for (int j = 0; j < 5; ++j) dma(rw, wo_[j], kwcol + ks * 64 * 3840, so + kwl + j * 1024);
  };
  const int tt = 4 * xcd + (iq >> 2), cq = iq & 3, t0 = tt * 64;
  if (isQ) {
    const unsigned r20 = lane / 20, so = (t0 + wv * 8) * 1280, la = L_QKIMG + 2576 + wv * 2576;
    unsigned vo = r20 * 1280 + (lane - r20 * 20) * 16 + cq * 320, xv = off_x(1); PINV(vo);
    MK_XW();
    if (wv == 0) warm(rx, xv, 0);
#define KVDELAY 16
    if (KVDELAY) __builtin_amdgcn_s_sleep(KVDELAY);
    const v4i rk = mk_rsrc(p.k, 2048 * 1280), rv = mk_rsrc(p.v, 2048 * 1280);
    if (lane < 60) {
      dma(rk, vo, so, la); dma(rk, vo, so + 3840, la + 960); dma(rv, vo, so, la + 8 * 2576); dma(rv, vo, so + 3840, la + 8 * 2576 + 960);
      if (wv == 7) dma(rk, tt ? vo + (t0 - 3) * 1280 : OOB, 0, L_QKIMG + 1600);
      if (lane < 40) {
        dma(rk, vo, so + 7680, la + 1920); dma(rv, vo, so + 7680, la + 8 * 2576 + 1920);
        if (wv == 5 || wv == 6) dma(mk_rsrc(p.sk, 640 * 8), cq * 1280 + (wv - 5) * 640 + lane * 16, 0, L_SW + (wv - 5) * 640);
      }
    }
  } else if (wv >= 4) { loader_offsets(); MK_XW(); issue_x(k0, L_KVSLOT); issue_kw(k0, L_KVSLOT); }
  else { const unsigned xv = off_x(0), wz = off_w(wv - 1); MK_XW(); warm(wv == 0 ? rx : rw, wv == 0 ? xv : wv < 3 ? wz : OOB, 0); }
  ST(12);
  asm volatile("" ::: "memory");
  const int c5 = 80 * g + 5 * rt, c0 = c5 + lw, cd = c5 + 4;
  const v4i rws = mk_rsrc(p.ws, WS_BYTES), ro = mk_rsrc(p.wo, 640 * 1280);
  const __amdgpu_buffer_rsrc_t bws = __builtin_amdgcn_make_buffer_rsrc(p.ws, 0, WS_BYTES, 0x00020000);
  const unsigned rep = (xcd & (NREP - 1)) * FL_N;
  const int l15 = lane & 15, grp = lane >> 4;
  const unsigned trl = (4 * grp + (l15 >> 2)) * 160 + 8 * (lane & 3);
    const int gr0 = (4 * xcd + ((bi >> 2) & 3)) * 64, gct = (bi & 3) + 4 * (bi >> 4), rot = (bi & 1) + 2 * (bi >> 4);
  const __amdgpu_buffer_rsrc_t bh = __builtin_amdgcn_make_buffer_rsrc((void*)p.h, 0, 640 * 4096, 0x00020000);

  auto h_store = [&](unsigned cb, int j, v4u d, v2u m) {
    const unsigned a = cb + 32 + j * 1024 + lane * 16;
    const unsigned e0 = __builtin_amdgcn_alignbit(m.y, m.x, 16), e1 = __builtin_amdgcn_alignbit(d.x, m.y, 16), e2 = __builtin_amdgcn_alignbit(d.y, d.x, 16),
                   e3 = __builtin_amdgcn_alignbit(d.z, d.y, 16), e4 = __builtin_amdgcn_alignbit(d.w, d.z, 16);
    sts128(a, (v4u){e0, e1, e2, e3}); sts128(a + L_CS, (v4u){m.y, d.x, d.y, d.z}); sts128(a + 2 * L_CS, (v4u){e1, e2, e3, e4}); sts128(a + 3 * L_CS, d);
  };
  auto h_pads = [&](unsigned cb) {
    if (lane < 38) sts128(lane < 30 ? cb + L_UOFF + 4096 + lane * 16 : cb + ((lane - 30) >> 1) * L_CS + (lane & 1) * 16, (v4u){0, 0, 0, 0});
  };
  auto load_u = [&](int c, unsigned cb) {
#pragma unroll
    for (int j = 0; j < 4; ++j) dma_sc1(rws, lane * 16, WS_UT + c * 4096 + j * 1024, cb + L_UOFF + j * 1024);
  };
  unsigned vo5[5];
  auto row_offsets = [&]() {
#pragma unroll
    for (int j = 0; j < 5; ++j) { const unsigned ch = j * 64 + lane, r = ch / 10, c = ch - r * 10; vo5[j] = r * 1280 + c * 16; PINV(vo5[j]); }
  };
  auto load_q = [&](int i) {
    const v4i rq = mk_rsrc(p.q, 2048 * 1280);
    int gb = gct; PINS(gb);
#pragma unroll
    for (int j = 0; j < 5; ++j) dma(rq, vo5[j], (gr0 + i * 32) * 1280 + gb * 160, L_QT + i * 5120 + j * 1024);
  };
  auto load_wo = [&](int i0, int i1, int step) {
    int gb = gct; PINS(gb);
#pragma unroll 1
    for (int i = i0; i < i1; i += step)
#pragma unroll
      for (int j = 0; j < 5; ++j) dma(ro, vo5[j], i * 32 * 1280 + gb * 160, L_WO + i * 5120 + j * 1024);
  };
  u64 tag; unsigned dbits, dbit5;
#define GET_TAG() do { const char* w_ = p.ws; const u16* d_ = p.Dv; PINS(w_); PINS(d_); u64 e_ = *(cu64*)w_; dbits = ((cu32*)d_)[c0 >> 1]; dbit5 = ((cu32*)d_)[cd >> 1]; PINS(e_); PINS(dbits); PINS(dbit5); \
    dbits = (dbits >> (16 * (c0 & 1))) & 0xffffu; dbit5 = (dbit5 >> (16 * (cd & 1))) & 0xffffu; tag = e_ * 6364136223846793005ull + 1442695040888963407ull; } while (0)

  if (!isQ) {
    if (wv < 4) {
      if (tid == 0) { asm volatile("" :: "v"(smem)); sts128(L_CNT, (v4u){0, 0, 0, 0}); sts128(L_CNT + 16, (v4u){0, 0, 0, 0}); sts128(L_CNT + 32, (v4u){0, 0, 0, 0}); sts128(L_CNT + 48, (v4u){0, 0, 0, 0}); }
      GET_TAG();
      PINS(tag);
      const int hf = wv & 1;
      const bool isk = !(l15 & 4), own = (l15 >> 2) == 2 * hf + 1;
      const unsigned pc0 = L_PC + wv * 896, trash = pc0 + 105 * 8;
      const int jb = own ? (l15 & 3) : l15 == 0 ? 4 : -1;
      unsigned pb = jb >= 0 ? pc0 + (80 + 5 * grp + jb) * 8 : trash, pcc = grp == 2 * hf ? pc0 + (64 + l15) * 8 : trash,
               pd = (grp == 2 * hf && jb >= 0) ? pc0 + (100 + jb) * 8 : trash;
      PINV(pb); PINV(pcc); PINV(pd);
      const ConvArgs ca = conv_args(L_KVCONV + wv * L_CH, dbits, WS_YT + c0 * 4096, lane);
      const ConvArgs c5a = conv_args(L_CONV5 + 128 * wv, wv ? 0u : dbit5, 0, lane);
      v4f aK[8], aV[8], aT[4];
#pragma unroll
      for (int m = 0; m < 8; ++m) { aK[m] = (v4f){1e-6f, 1e-6f, 1e-6f, 1e-6f}; aV[m] = (v4f){0, 0, 0, 0}; }
#pragma unroll
      for (int m = 0; m < 4; ++m) { const float c = isk ? 1e-6f : 0.f; aT[m] = (v4f){c, c, c, c}; }
      const unsigned x0 = l15 * 128 + ((grp ^ (l15 >> 1)) << 4), trr = (4 * grp + (l15 >> 2)) * 160;
      gemm_kv(aK, aV, aT, x0 + hf * 8192, x0 + (1 - hf) * 8192, 16384 + trl + wv * 40, 16384 + trr + (lane & 1) * 10256 + (wv >> 1) * 80 + (lane & 2) * 20 + 32 STPASS);
      v4u tf[4], kf[4], vf[4];
#pragma unroll
      for (int t = 0; t < 2; ++t) {
        const v4u a = cat(phi4(aT[2 * t]), phi4(aT[2 * t + 1])), c = cat(pk4(aT[2 * t]), pk4(aT[2 * t + 1]));
#pragma unroll
        for (int d = 0; d < 4; ++d) tf[t][d] = isk ? a[d] : c[d];
        sts128(L_KVSLOT + wv * 2048 + t * 1024 + lane * 16, tf[t]);
      }
      asm volatile("" ::: "memory");
      sts32(L_CNT + 32 + wv * 4, 1u);
      v4f dA = {0, 0, 0, 0}, dB = dA, dC = dA, dD = dA;
      auto kvstep = [&](int t) {
        v4u tb;
#pragma unroll
        for (int d = 0; d < 4; ++d) tb[d] = own ? tf[t][d] : 0x3f803f80u;
        dA = mfma(kf[t], vf[t], dA); dB = mfma(kf[t], tb, dB); dC = mfma(tf[t], vf[t], dC); dD = mfma(tf[t], tb, dD);
      };
#pragma unroll
      for (int t = 0; t < 3; ++t) { kf[t] = cat(phi4(aK[2 * t]), phi4(aK[2 * t + 1])); vf[t] = cat(pk4(aV[2 * t]), pk4(aV[2 * t + 1])); }
      kvstep(0);
      PINV(kf[2]); PINV(vf[2]);
      asm volatile("" ::: "memory");
      unsigned mark = lds32(L_CNT + 32 + (wv ^ 1) * 4);
      asm volatile("" ::: "memory");
      tf[2] = lds128(L_KVSLOT + (wv ^ 1) * 2048 + lane * 16); tf[3] = lds128(L_KVSLOT + (wv ^ 1) * 2048 + 1024 + lane * 16);
      asm volatile("" ::: "memory");
      kf[3] = cat(phi4(aK[6]), phi4(aK[7])); vf[3] = cat(pk4(aV[6]), pk4(aV[7]));
      kvstep(1);
      PINV(kf[3]); PINV(vf[3]);
      if (__builtin_amdgcn_readfirstlane(mark) == 0) {
        lds_wait<0>(L_CNT + 32 + (wv ^ 1) * 4, 1);
        tf[2] = lds128(L_KVSLOT + (wv ^ 1) * 2048 + lane * 16); tf[3] = lds128(L_KVSLOT + (wv ^ 1) * 2048 + 1024 + lane * 16);
      }
      kvstep(2); kvstep(3);
      sts64(pc0 + lane * 8, pk4(dA)); sts64(pb, pk4(dB)); sts64(pcc, pk4(dC)); sts64(pd, pk4(dD));
      asm volatile("" ::: "memory");
      sts32(L_CNT + 32 + wv * 4, 2u);
      ST(2);
      lds_wait<0>(L_CNT + 16, 4);
      ST(4);
      conv_wave(c5a, ca, p.ws, wv, lane STPASS);
      ST(5);
    } else {
      auto piece = [&](int q) {
        const int st = q / 9, j = q % 9; const int ks = k0 + st >= 10 ? k0 + st - 10 : k0 + st; const unsigned so = ((st & 3) ^ 1) * L_KVSLOT;
        if (j < 4) dma(rx, (j & 1) ? xo1 : xo0, ks * 128 + (j >> 1) * 16 * 1280, so + lw * 4096 + j * 1024);
        else dma(rw, wo_[j - 4], kwcol + ks * 64 * 3840, so + kwl + (j - 4) * 1024);
      };
      GET_TAG();
      row_offsets();
      unsigned yo5 = WS_YT + cd * 4096 + 16 * l15 + 256 * (grp >> 1); PINV(yo5);
      unsigned wov = lane < 50 ? (bi * 200 + lw * 50 + lane) * 128 : OOB; PINV(wov);
      unsigned pso = lane < 56 ? WS_PART + (g * 16 + rt) * PART_STRIDE + lw * 896 + lane * 16 : OOB; PINV(pso);
#define KVSTEP(s, q0, q1) do { _Pragma("unroll") for (int q = (q0); q < (q1); ++q) piece(q); waitv<(q1) - 9 * ((s) + 1)>(); rawbar(); } while (0)
      KVSTEP(0, 9, 13); ST(14);
      KVSTEP(1, 13, 23); KVSTEP(2, 23, 33); KVSTEP(3, 33, 43); KVSTEP(4, 43, 53);
      KVSTEP(5, 53, 63); KVSTEP(6, 63, 73); KVSTEP(7, 73, 83); KVSTEP(8, 83, 90);
#undef KVSTEP
      ST(15);
      const unsigned cb = L_KVCONV + lw * L_CH + 32;
      {
        const v4i rhs = mk_rsrc((const char*)p.h - 16, 640 * 4096 + 16);
#pragma unroll
        for (int j = 0; j < 4; ++j) dma(rhs, lane * 16, c0 * 4096 + j * 1024 + 16, cb + 3 * L_CS + j * 1024);
#pragma unroll
        for (int j = 1; j < 4; ++j) dma(rhs, lane * 16, c0 * 4096 + j * 1024 + 12, cb + L_CS + j * 1024);
      }
      h_pads(L_KVCONV + lw * L_CH);
      waitv<7>(); rawbar();
      ST(13);
      __builtin_amdgcn_s_setprio(3);
      v4u hd5 = __builtin_amdgcn_raw_buffer_load_b128(bh, cd * 4096 + lw * 1024 + lane * 16, 0, 0);
      v2u hm5 = __builtin_amdgcn_raw_buffer_load_b64(bh, (lw == 0 && lane == 0) ? OOB : (unsigned)(cd * 4096 + lw * 1024 + lane * 16 - 8), 0, 0);
      SCHED();
      wait_flags(p.ws, rep + FL_U + (g >> 1) * 32 + lw * 8, 8, tag, lane);
      waitv<0>();
      PINV(hd5); PINV(hm5);
      lds_arrive(L_CNT + 28, lane);
      if (wv == 7) h_pads(L_CONV5);
      lds_wait<0>(L_CNT + 28, 4);
      dma_sc1(rws, lane * 16, WS_UT + cd * 4096 + lw * 1024, L_CONV5 + L_UOFF + lw * 1024);
      load_u(c0, L_KVCONV + lw * L_CH);
      lds_arrive(L_CNT + 28, lane);
      h_store(L_CONV5, lw, hd5, hm5);
      lds_wait<0>(L_CNT + 28, 8);
      warm(ro, wov, 3200 * 128); warm(ro, wov, 0);
      waitv<6>();
      lds_wait<1>(L_CNT + 32 + lw * 4, 1);
      lds_arrive(L_CNT + 16, lane);
      ST(1);
      v4u hd[4]; v2u hm[4];
#pragma unroll
      for (int j = 0; j < 4; ++j) { hd[j] = lds128(cb + 3 * L_CS + j * 1024 + lane * 16); hm[j] = lds64(cb + 3 * L_CS + j * 1024 + lane * 16 - 8); }
      waitv<2>();
      lds_wait<PUBSLEEP>(L_CNT + 32 + lw * 4, 2);
      __builtin_amdgcn_raw_buffer_store_b128(lds128(L_PC + lw * 896 + lane * 16), bws, pso, 0, 16);
      SCHED();
#pragma unroll
      for (int j = 0; j < 4; ++j) {
        const unsigned a = cb + j * 1024 + lane * 16; const v4u d = hd[j]; const v2u m = hm[j];
        const unsigned e0 = __builtin_amdgcn_alignbit(m.y, m.x, 16), e1 = __builtin_amdgcn_alignbit(d.x, m.y, 16), e2 = __builtin_amdgcn_alignbit(d.y, d.x, 16),
                       e3 = __builtin_amdgcn_alignbit(d.z, d.y, 16), e4 = __builtin_amdgcn_alignbit(d.w, d.z, 16);
        sts128(a, (v4u){e0, e1, e2, e3}); sts128(a + 2 * L_CS, (v4u){e1, e2, e3, e4});
        if (j == 0) sts128(a + L_CS, (v4u){m.y, d.x, d.y, d.z});
      }
      asm volatile("" ::: "memory");
      sts32(L_CNT + 48 + lw * 4, 1u);
      ST(16);
      arrive_flag(L_CNT + 8, 4, p.ws, FL_P + g * 16 + rt, tag, lane);
      ST(12);
      lds_wait<1>(L_CNT + 8, 4);
      warm(mk_rsrc(p.q, 2048 * 1280), (gr0 + lw * 16 + (lane >> 2)) * 1280 + gct * 160 + (lane & 2) * 320 + (lane & 1) * 156, 0);
      lds_wait<2>(L_CNT + 12, 4);
      ST(5);
      v4f ya = __builtin_bit_cast(v4f, lds128(L_RED + lw * 4096 + lane * 16)), yb = __builtin_bit_cast(v4f, lds128(L_RED + (7 - lw) * 4096 + lane * 16));
#pragma unroll
      for (int w = 1; w < 4; ++w) { ya = add4(ya, __builtin_bit_cast(v4f, lds128(L_RED + (lw * 4 + w) * 1024 + lane * 16))); yb = add4(yb, __builtin_bit_cast(v4f, lds128(L_RED + ((7 - lw) * 4 + w) * 1024 + lane * 16))); }
      conv_store(conv_pack(ya, yb), lw, yo5, p.ws, lane);
      lds_arrive(L_CNT + 12, lane);
      lds_wait<2>(L_CNT + 12, 8);
      ST(6);
      load_wo(lw, 12, 4);
      ST(7);
      if (lw < 2) load_q(lw);
      ST(8);
      waitv<0>();
      ST(17);
      bar();
      load_wo(12 + lw, 20, 4);
      __builtin_amdgcn_s_setprio(0);
      waitv<0>();
      lds_arrive(L_CNT + 20, lane);
    }
  } else {
    if (tid == 0) { asm volatile("" :: "v"(smem)); sts128(L_CNT, (v4u){0, 0, 0, 0}); sts128(L_CNT + 16, (v4u){0, 0, 0, 0}); }
    GET_TAG();
    if (wv < 4) {
      waitv<0>(); bar();
      ST(1);
      {
        const int tg = lane & 7;
        unsigned ka = L_QKIMG + tg * 2576 + (8 * wv + (lane >> 3)) * 4, sa = L_SW + (8 * wv + (lane >> 3)) * 16;
        unsigned uo = WS_UT + (160 * cq + 16 * wv + 2 * (lane >> 3)) * 4096 + 2 * (2040 - t0 - 8 * tg);
        unsigned kr[11], vr[8]; v4u sw;
#define PRD() do { PINV(ka); sw = lds128(sa); _Pragma("unroll") for (int j = 0; j < 11; ++j) kr[j] = lds32(ka + (j < 3 ? (5 + j) * 320 : 2576 + (j - 3) * 320)); \
          _Pragma("unroll") for (int j = 0; j < 8; ++j) vr[j] = lds32(ka + (L_QVIMG - L_QKIMG) + j * 320); ka += 128; sa += 512; } while (0)
        PRD();
        v4u ua[3], ub[3];
#pragma unroll
        for (int it = 0; it < 3; ++it) {
          if (it == 2 && wv >= 2) break;
          v2f kk[11], vv[8];
#pragma unroll
          for (int j = 0; j < 11; ++j) kk[j] = (v2f){blo(kr[j]), bhi(kr[j])};
#pragma unroll
          for (int j = 0; j < 8; ++j) vv[j] = (v2f){blo(vr[j]), bhi(vr[j])};
          const v2f w0 = {blo(sw.x), blo(sw.z)}, w1 = {bhi(sw.x), bhi(sw.z)}, w2 = {blo(sw.y), blo(sw.w)}, w3 = {bhi(sw.y), bhi(sw.w)};
          SCHED();
          if (it < 2) PRD();
          SCHED();
          v2f uu[8];
#pragma unroll
          for (int j = 0; j < 8; ++j) uu[j] = (kk[j] * w3 + kk[j + 1] * w2 + kk[j + 2] * w1 + kk[j + 3] * w0) * vv[j];
          ua[it] = (v4u){pk2(uu[7].x, uu[6].x), pk2(uu[5].x, uu[4].x), pk2(uu[3].x, uu[2].x), pk2(uu[1].x, uu[0].x)};
          ub[it] = (v4u){pk2(uu[7].y, uu[6].y), pk2(uu[5].y, uu[4].y), pk2(uu[3].y, uu[2].y), pk2(uu[1].y, uu[0].y)};
          if (it == 2) { __builtin_amdgcn_raw_buffer_store_b128(ua[2], bws, uo, 128 * 4096, 16); __builtin_amdgcn_raw_buffer_store_b128(ub[2], bws, uo, 129 * 4096, 16); }
        }
        ST(13);
#pragma unroll
        for (int it = 0; it < 2; ++it) { __builtin_amdgcn_raw_buffer_store_b128(ua[it], bws, uo, it * 64 * 4096, 16); __builtin_amdgcn_raw_buffer_store_b128(ub[it], bws, uo, (it * 64 + 1) * 4096, 16); }
#undef PRD
      }
      ST(2);
      unsigned ka[13], za[5];
      {
        int n = 0;
#pragma unroll
        for (int a = 0; a < 5; ++a) {
          const int Dq = 4 * a + grp, hh = Dq / 5, dq = Dq - 5 * hh, eb = l15 - 20 * hh;
          za[a] = L_KVC + ((hh == (l15 & 3)) ? hh * 112 + 84 + 5 * dq : 448) * 8; PINV(za[a]);
#pragma unroll
          for (int bb = 0; bb < 5; ++bb) if (bb >= a - 1 && bb <= a + 1) { const int e = 16 * bb + eb; ka[n] = L_KVC + (((unsigned)e < 16u) ? hh * 112 + 16 * dq + e : ((unsigned)e < 20u) ? hh * 112 + 64 + 5 * dq + e : 448) * 8; PINV(ka[n]); ++n; }
        }
      }
      unsigned po = (tid < 224) ? WS_PART + g * 16 * PART_STRIDE + tid * 16 : OOB; PINV(po);
      v4f acc[2][5];
#pragma unroll
      for (int m = 0; m < 2; ++m)
#pragma unroll
        for (int n = 0; n < 5; ++n) acc[m][n] = (v4f){1e-6f, 1e-6f, 1e-6f, 1e-6f};
      const unsigned xa = (wv * 32 + l15) * 128 + ((grp ^ (l15 >> 1)) << 4);
      gemm_q(acc, xa, 16384 + trl, p.ws, FL_U + cq * 32 + tt, tag, lane);
      __builtin_amdgcn_s_setprio(2);
      ST(3);
      v2u pq[2][5];
#pragma unroll
      for (int m = 0; m < 2; ++m)
#pragma unroll
        for (int n = 0; n < 5; ++n) { pq[m][n] = phi4(acc[m][n]); PINV(pq[m][n]); }
      ST(4);
      lds_wait<1>(L_CNT + 20, 4);
      wait_flags(p.ws, rep + FL_P + g * 16, 16, tag, lane);
      ST(6);
      {
        v4u r[16];
#pragma unroll
        for (int j = 0; j < 16; ++j) { r[j] = __builtin_amdgcn_raw_buffer_load_b128(bws, po, ((j + 4 * bi) & 15) * PART_STRIDE, 16); SCHED(); }
        v4f s0 = {0, 0, 0, 0}, s1 = s0;
        unsigned sel0 = 0x00003f80u, sel1 = 0x3f800000u; PINV(sel0); PINV(sel1);
        auto dot = [](unsigned x, unsigned sel, float c) { return __builtin_amdgcn_fdot2_f32_bf16(__builtin_bit_cast(v2bf, x), __builtin_bit_cast(v2bf, sel), c, false); };
#pragma unroll
        for (int j = 0; j < 16; ++j) { s0[0] = dot(r[j].x, sel0, s0[0]); s0[1] = dot(r[j].x, sel1, s0[1]); s0[2] = dot(r[j].y, sel0, s0[2]); s0[3] = dot(r[j].y, sel1, s0[3]);
          s1[0] = dot(r[j].z, sel0, s1[0]); s1[1] = dot(r[j].z, sel1, s1[1]); s1[2] = dot(r[j].w, sel0, s1[2]); s1[3] = dot(r[j].w, sel1, s1[3]); }
        sts128(L_KVC + tid * 16, cat(pk4(s0), pk4(s1)));
      }
      ST(17);
      lds_arrive(L_CNT + 8, lane); lds_wait<0>(L_CNT + 8, 4);
      ST(7);
      {
        const v2u z2 = {0, 0};
        const v4u z0 = cat(lds64(za[0]), lds64(za[1])), z1 = cat(lds64(za[2]), lds64(za[3])), zz = cat(lds64(za[4]), z2);
        const v4u a0 = cat(lds64(ka[0]), lds64(ka[2])), a1 = cat(lds64(ka[1]), lds64(ka[3])), b1 = cat(lds64(ka[5]), z2), a2 = cat(z2, lds64(ka[4])), b2 = cat(lds64(ka[6]), lds64(ka[8])),
                  b3 = cat(lds64(ka[7]), lds64(ka[9])), c3 = cat(lds64(ka[11]), z2), b4 = cat(z2, lds64(ka[10])), c4 = cat(lds64(ka[12]), z2);
        const v4f zero = {0, 0, 0, 0};
#pragma unroll
        for (int m = 0; m < 2; ++m) {
          const v4u s0 = cat(pq[m][0], pq[m][1]), s1 = cat(pq[m][2], pq[m][3]), s2 = cat(pq[m][4], z2);
          v4f den = mfma(z0, s0, zero); den = mfma(z1, s1, den); den = mfma(zz, s2, den);
          v4f n0 = mfma(a0, s0, zero);
          v4f n1 = mfma(a1, s0, zero); n1 = mfma(b1, s1, n1);
          v4f n2 = mfma(a2, s0, zero); n2 = mfma(b2, s1, n2);
          v4f n3 = mfma(b3, s1, zero); n3 = mfma(c3, s2, n3);
          v4f n4 = mfma(b4, s1, zero); n4 = mfma(c4, s2, n4);
          const float r0 = __builtin_amdgcn_rcpf(den[0] + 1e-6f), r1 = __builtin_amdgcn_rcpf(den[1] + 1e-6f), r2 = __builtin_amdgcn_rcpf(den[2] + 1e-6f), r3 = __builtin_amdgcn_rcpf(den[3] + 1e-6f);
          n0 *= r0; n1 *= (grp < 1) ? r0 : r1; n2 *= (grp < 2) ? r1 : r2; n3 *= (grp < 3) ? r2 : r3; n4 *= r3;
          const unsigned oo = WS_OF + ((rt * 8 + 2 * wv + m) * 8 + g) * 2560;
          __builtin_amdgcn_raw_buffer_store_b128(cat(pk4(n0), pk4(n1)), bws, lane * 16, oo, 16);
          __builtin_amdgcn_raw_buffer_store_b128(cat(pk4(n2), pk4(n3)), bws, lane * 16 + 1024, oo, 16);
          __builtin_amdgcn_raw_buffer_store_b64(pk4(n4), bws, lane * 8 + 2048, oo, 16);
        }
        ST(8);
        lds_arrive(L_CNT + 16, lane);
        waitv<0>();
        set_flag(p.ws, FL_O + (rt * 4 + wv) * 8 + g, tag, 1, lane);
        __builtin_amdgcn_s_setprio(0);
      }
    } else {
      if (lw == 2) warm_x(2); else warm_w(2 + lw - (lw >> 1));
      loader_offsets(); row_offsets();
      unsigned w1o = (lw & 1) ? off_w(5 + (lw >> 1)) : off_x(3 + (lw >> 1)), wqo;
      { const unsigned r5 = lane / 5, rho = 12 * lw + r5; int kb = k0 + (rho >> 2); kb = kb >= 10 ? kb - 10 : kb;
        wqo = (lane < 60 && rho < 40) ? (kb * 64 + iq * 4 + (rho & 3)) * 3840 + (xcd & 1) * 640 + (lane - r5 * 5) * 128 : OOB; PINV(wqo); }
      waitv<1>(); bar();
      warm((lw & 1) ? rw : rx, w1o, 0);
      warm(rw, wqo, 0);
      ST(14);
      const unsigned wcol = 160 * g + (lw >> 1) * 32 * 3840, wl = 16384 + (lw >> 1) * 5120;
      const unsigned w0 = (lw & 1) ? wo_[1] : wo_[0], w1 = (lw & 1) ? wo_[3] : wo_[2];
      int ks = k0;
      unsigned so = 0;
#pragma unroll 1
      for (int s = -1; s < 9; ++s) {
        issue_x(ks, so);
        dma(rw, w0, wcol + ks * 64 * 3840, so + wl + (lw & 1) * 1024);
        dma(rw, w1, wcol + ks * 64 * 3840, so + wl + (lw & 1) * 1024 + 2048);
        if (!(lw & 1)) dma(rw, wo_[4], wcol + ks * 64 * 3840, so + wl + 4096);
        ks = ks == 9 ? 0 : ks + 1; so = (so == 3 * L_QSLOT) ? 0 : so + L_QSLOT;
        if (s >= 0) { if (lw & 1) waitv<6>(); else waitv<7>(); rawbar(); __builtin_amdgcn_s_setprio(1); }
        if (s == 0) ST(13);
        if (s == 1) ST(2);
        if (s == 4) ST(3);
      }
      if (lw < 3) { load_wo(10 + lw, 13, 4); waitv<5>(); } else waitv<0>();
      rawbar();
      ST(4);
      load_wo(16 - lw, 20, 4);
      lds_wait<1>(L_CNT + 28, 4);
      load_wo(lw, 10, 4);
      waitv<0>();
      lds_arrive(L_CNT + 20, lane);
      __builtin_amdgcn_s_setprio(0);
      ST(6);
      lds_wait<4>(L_CNT + 16, 4);
      if (lw < 2) load_q(lw);
      waitv<0>();
      lds_arrive(L_CNT, lane); lds_wait<1>(L_CNT, 4);
    }
  }
  ST(9);
#define AUX_B 0
#define AUX_A 0
  const __amdgpu_buffer_rsrc_t bout = __builtin_amdgcn_make_buffer_rsrc(p.out, 0, 2048 * 2560, 0x00020000);
  unsigned oo3[3], sta = (wv < 4 ? L_ST : L_QT) + lw * 2560;
#pragma unroll
  for (int j = 0; j < 3; ++j) { const unsigned ch = j * 64 + lane, r = ch / 10;
    oo3[j] = ch < 160 ? (gr0 + lw * 16 + r) * 2560 + (wv < 4 ? 1280 : 0) + gct * 160 + (ch - r * 10) * 16 : OOB; PINV(oo3[j]); }
  unsigned stw = sta + l15 * 160 + grp * 8; PINV(stw);
  v2u res[5];
  if (wv < 4) {
    const int mt = (gr0 >> 4) + wv;
    unsigned wbq[4];
#pragma unroll
    for (int y = 0; y < 4; ++y) { wbq[y] = L_WO + trl + (y ^ rot) * 25600; PINV(wbq[y]); }
    v4u wf[7][5];
#define WRD(t, n) do { const int y_ = (t) / 5, s_ = (t) % 5; const unsigned ra = (s_ == 4) ? 64 : (s_ >> 1) * 80 + (s_ & 1) * 32, rb = (s_ == 4) ? 144 : ra + 16; \
      wf[(t) % 7][n] = cat(ldtr(wbq[y_] + ra * 160 + (n) * 32), ldtr(wbq[y_] + rb * 160 + (n) * 32)); } while (0)
    v2u f0 = __builtin_amdgcn_raw_buffer_load_b64(bws, lane < 8 ? lane * 128 : OOB, WS_FLAG + (FL_O + (mt >> 1) * 8) * 128, 16);
    SCHED();
#pragma unroll
    for (int f = 0; f < 35; ++f) WRD(f / 5, f % 5);
    SCHED();
    if (!isQ) arrive_flag(L_CNT + 4, 4, p.ws, FL_Y + g * 16 + rt, tag, lane);
    lds_wait<1>(L_CNT + 20, 4);
    if (b == 0 && wv == 0) { wait_flags(p.ws, FL_U, 64, tag, lane); wait_flags(p.ws, FL_U + 64, 64, tag, lane); }
    PINV(f0);
    if (!__all(lane >= 8 || (f0.x == (unsigned)tag && f0.y == (unsigned)(tag >> 32)))) wait_flags(p.ws, FL_O + (mt >> 1) * 8, 8, tag, lane);
    asm volatile("" ::: "memory");
    ST(10);
    v4u of[20];
#define ORD(t) do { const int s_ = (t) % 5; const unsigned oo = WS_OF + (mt * 8 + 2 * (((t) / 5) ^ rot)) * 2560; \
      if (s_ < 4) of[t] = __builtin_amdgcn_raw_buffer_load_b128(bws, lane * 16 + (s_ & 1) * 1024, oo + (s_ >> 1) * 2560, 16); \
      else of[t] = cat(__builtin_amdgcn_raw_buffer_load_b64(bws, lane * 8 + 2048, oo, 16), __builtin_amdgcn_raw_buffer_load_b64(bws, lane * 8 + 2048, oo + 2560, 16)); } while (0)
#define OAHEAD 12
#pragma unroll
    for (int t = 0; t < OAHEAD; ++t) { ORD(t); SCHED(); }
    if (b == 0 && tid == 0) __hip_atomic_store((GAS u64*)p.ws, tag, RLX_AGENT);
    SCHED();
    v4f acc[5];
#pragma unroll
    for (int n = 0; n < 5; ++n) acc[n] = (v4f){0, 0, 0, 0};
#pragma unroll
    for (int t = 0; t < 20; ++t) {
      v4u cur[5];
#pragma unroll
      for (int n = 0; n < 5; ++n) cur[n] = wf[t % 7][n];
#pragma unroll
      for (int f = 0; f < 65; ++f) if (t >= 1 && f >= (t - 1) * 15 / 4 && f < t * 15 / 4) WRD(7 + f / 5, f % 5);
#pragma unroll
      for (int n = 0; n < 5; ++n) acc[n] = mfma(cur[n], of[t], acc[n]);
      if (t + OAHEAD < 20) ORD(t + OAHEAD);
      __builtin_amdgcn_sched_group_barrier(0x8, 1, 0); __builtin_amdgcn_sched_group_barrier(0x100, 2, 0);
      __builtin_amdgcn_sched_group_barrier(0x8, 1, 0); __builtin_amdgcn_sched_group_barrier(0x100, 1, 0); __builtin_amdgcn_sched_group_barrier(0x20, 1, 0);
      __builtin_amdgcn_sched_group_barrier(0x8, 1, 0); __builtin_amdgcn_sched_group_barrier(0x100, 2, 0);
      __builtin_amdgcn_sched_group_barrier(0x8, 1, 0); __builtin_amdgcn_sched_group_barrier(0x100, 1, 0); __builtin_amdgcn_sched_group_barrier(0x20, 1, 0);
      __builtin_amdgcn_sched_group_barrier(0x8, 1, 0); __builtin_amdgcn_sched_group_barrier(0x100, 2, 0);
      SCHED();
    }
#undef WRD
#undef ORD
#pragma unroll
    for (int n = 0; n < 5; ++n) res[n] = pk4(acc[n]);
  } else {
    v4f qf[5];
#pragma unroll
    for (int n = 0; n < 5; ++n) { const v2u u = lds64(stw + n * 32); qf[n] = (v4f){blo(u.x), bhi(u.x), blo(u.y), bhi(u.y)}; PINV(qf[n]); }
    unsigned yo = (lane >> 3) * 4096 + (lane & 3) * 16 + (lane & 4) * 64; PINV(yo);
    unsigned ya = L_YT + (4 * grp + (l15 >> 2)) * 128 + (lane & 2) * 32 + lw * 16 + (lane & 1) * 8; PINV(ya);
    const unsigned ys = WS_YT + (80 * gct + 20 * lw) * 4096 + (gr0 >> 8) * 512 + (gr0 & 192);
    wait_flags(p.ws, rep + FL_Y + gct * 16 + lw * 4, 4, tag, lane);
    ST(10);
    dma_sc1(rws, yo, ys, L_YT + lw * 2560); dma_sc1(rws, yo, ys + 8 * 4096, L_YT + lw * 2560 + 1024);
    if (lane < 32) dma_sc1(rws, yo, ys + 16 * 4096, L_YT + lw * 2560 + 2048);
    waitv<0>();
    lds_arrive(L_CNT + 24, lane); lds_wait<1>(L_CNT + 24, 4);
#pragma unroll
    for (int n = 0; n < 5; ++n) { const v2u u = ldtr(ya + n * 2048); res[n] = pk4(mul4(qf[n], (v4f){blo(u.x), bhi(u.x), blo(u.y), bhi(u.y)})); }
  }
#pragma unroll
  for (int n = 0; n < 5; ++n) sts64(stw + n * 32, res[n]);
  asm volatile("" ::: "memory");
#pragma unroll
  for (int j = 0; j < 3; ++j) __builtin_amdgcn_raw_buffer_store_b128(lds128(sta + j * 1024 + lane * 16), bout, oo3[j], 0, AUX_B);
  ST(11);
}

extern "C" void kernel_launch(void* const* d_in, const int* in_sizes, int n_in, void* d_out, int out_size, void* d_ws, size_t ws_size, hipStream_t stream) {
  Params p;
  p.q = (const u16*)d_in[0]; p.k = (const u16*)d_in[1]; p.v = (const u16*)d_in[2]; p.sk = (const u16*)d_in[3]; p.h = (const u16*)d_in[4];
  p.Dv = (const u16*)d_in[5]; p.x = (const u16*)d_in[6]; p.w = (const u16*)d_in[7]; p.wo = (const u16*)d_in[8];
  p.out = (u16*)d_out; p.ws = (char*)d_ws;
  fused_kernel<<<256, 512, 0, stream>>>(p);
}
```

```cpp
#include <hip/hip_runtime.h>
typedef unsigned short u16;
typedef unsigned long long u64;
typedef unsigned v2u __attribute__((ext_vector_type(2)));
typedef unsigned v4u __attribute__((ext_vector_type(4)));
typedef int v4i __attribute__((ext_vector_type(4)));
typedef float v2f __attribute__((ext_vector_type(2)));
typedef float v4f __attribute__((ext_vector_type(4)));
typedef short v4s __attribute__((ext_vector_type(4)));
typedef __bf16 v2bf __attribute__((ext_vector_type(2)));
typedef __bf16 v8bf __attribute__((ext_vector_type(8)));
#define GAS __attribute__((address_space(1)))
#define LAS __attribute__((address_space(3)))
#define RLX_AGENT __ATOMIC_RELAXED, __HIP_MEMORY_SCOPE_AGENT
#define DEVINL __device__ __forceinline__

constexpr unsigned WS_FLAG = 4096;
constexpr unsigned FL_U = 0, FL_P = 128, FL_Y = 256, FL_O = 512, FL_P2 = 1024;
constexpr unsigned FL_N = 1152;
constexpr unsigned WS_UT = 2u << 20, WS_YT = 5u << 20, WS_PART = 8u << 20, WS_OF = 9u << 20, WS_STAMP = 12u << 20;
constexpr unsigned WS_BYTES = 13u << 20;
constexpr unsigned PART_STRIDE = 3584;
constexpr unsigned OOB = 0x80000000u;
constexpr unsigned L_KVSLOT = 36992, L_WV = 16384 + 10256, L_QSLOT = 26624;
constexpr unsigned L_CS = 4160, L_UOFF = 4 * L_CS, L_CH = 21248;
constexpr unsigned L_KVCONV = 73984, L_QKIMG = 79872, L_QVIMG = 79872 + 9 * 2576, L_QCONV = 123904;
constexpr unsigned L_YT = 113664, L_ST = 146432, L_KVC = 156672, L_PC = 158976, L_DUMMY = 162816, L_CNT = 163584;
constexpr unsigned L_QT = 0, L_WO = 11264, L_SW = 161280;
#define CODE_BYTES 43000

struct Params { const u16 *q, *k, *v, *sk, *h, *Dv, *x, *w, *wo; u16* out; char* ws; };

DEVINL v4i mk_rsrc(const void* p, unsigned bytes) {
  u64 a = (u64)p; v4i r; r.x = (int)(unsigned)a; r.y = (int)((unsigned)(a >> 32) & 0xffffu); r.z = (int)bytes; r.w = 0x00020000; return r;
}
DEVINL void dma(v4i rs, unsigned voff, unsigned soff, unsigned lds) {
  asm volatile("s_mov_b32 m0, %3\n\ts_nop 0\n\tbuffer_load_dwordx4 %0, %1, %2 offen lds" :: "v"(voff), "s"(rs), "s"(soff), "s"(lds) : "memory");
}
DEVINL void warm_sc1(v4i rs, unsigned voff, unsigned soff) {
  asm volatile("s_mov_b32 m0, %3\n\ts_nop 0\n\tbuffer_load_dword %0, %1, %2 offen sc1 lds" :: "v"(voff), "s"(rs), "s"(soff), "s"(L_DUMMY) : "memory");
}
DEVINL void dma_sc1(v4i rs, unsigned voff, unsigned soff, unsigned lds) {
  asm volatile("s_mov_b32 m0, %3\n\ts_nop 0\n\tbuffer_load_dwordx4 %0, %1, %2 offen sc1 lds" :: "v"(voff), "s"(rs), "s"(soff), "s"(lds) : "memory");
}
DEVINL void warm(v4i rs, unsigned voff, unsigned soff) {
  asm volatile("s_mov_b32 m0, %3\n\ts_nop 0\n\tbuffer_load_dword %0, %1, %2 offen lds" :: "v"(voff), "s"(rs), "s"(soff), "s"(L_DUMMY) : "memory");
}
template <int N> DEVINL void waitv() { asm volatile("s_waitcnt vmcnt(%0)" :: "n"(N) : "memory"); }
DEVINL void rawbar() { asm volatile("s_barrier" ::: "memory"); }
DEVINL void bar() { asm volatile("s_waitcnt lgkmcnt(0)\n\ts_barrier" ::: "memory"); }
#define SCHED() __builtin_amdgcn_sched_barrier(0)
DEVINL v4u lds128(unsigned a) { return *(const LAS v4u*)(size_t)a; }
DEVINL v2u lds64(unsigned a) { return *(const LAS v2u*)(size_t)a; }
DEVINL unsigned lds32(unsigned a) { return *(const LAS unsigned*)(size_t)a; }
DEVINL void sts128(unsigned a, v4u v) { *(LAS v4u*)(size_t)a = v; }
DEVINL void sts64(unsigned a, v2u v) { *(LAS v2u*)(size_t)a = v; }
DEVINL void sts32(unsigned a, unsigned v) { *(LAS unsigned*)(size_t)a = v; }
DEVINL v2u ldtr(unsigned a) { return __builtin_bit_cast(v2u, __builtin_amdgcn_ds_read_tr16_b64_v4i16((LAS v4s*)(size_t)a)); }
DEVINL v4u cat(v2u a, v2u b) { return (v4u){a.x, a.y, b.x, b.y}; }
DEVINL v4f mfma(v4u a, v4u b, v4f c) { return __builtin_amdgcn_mfma_f32_16x16x32_bf16(__builtin_bit_cast(v8bf, a), __builtin_bit_cast(v8bf, b), c, 0, 0, 0); }
DEVINL unsigned pk2(float a, float b) { v2f p = {a, b}; return __builtin_bit_cast(unsigned, __builtin_convertvector(p, v2bf)); }
DEVINL v2u pk4(v4f a) { return (v2u){pk2(a[0], a[1]), pk2(a[2], a[3])}; }
DEVINL float blo(unsigned u) { return __uint_as_float(u << 16); }
DEVINL float bhi(unsigned u) { return __uint_as_float(u & 0xffff0000u); }
DEVINL unsigned phi2(unsigned u) {
  const v2f t = {blo(u), bhi(u)};
  const v2f a = t * 1.44269504089f, c = t + 1e-6f;
  const v2f e = {__builtin_amdgcn_fmed3f(__builtin_amdgcn_exp2f(a.x), 0.f, 1.f), __builtin_amdgcn_fmed3f(__builtin_amdgcn_exp2f(a.y), 0.f, 1.f)};
  const v2f m = {fmaxf(c.x, 1e-6f), fmaxf(c.y, 1e-6f)};
  const v2f r = m + e;
  return pk2(r.x, r.y);
}
DEVINL v2u phi4(v4f c) {
  const v4f a = c * 1.44269504089f - 1.44269504089e-6f;
  v4f r;
#pragma unroll
  for (int j = 0; j < 4; ++j) r[j] = __int_as_float(max(__float_as_int(c[j]), 0x358637bd));
  const v4f e = {__builtin_amdgcn_fmed3f(__builtin_amdgcn_exp2f(a[0]), 0.f, 1.f), __builtin_amdgcn_fmed3f(__builtin_amdgcn_exp2f(a[1]), 0.f, 1.f),
                 __builtin_amdgcn_fmed3f(__builtin_amdgcn_exp2f(a[2]), 0.f, 1.f), __builtin_amdgcn_fmed3f(__builtin_amdgcn_exp2f(a[3]), 0.f, 1.f)};
  return pk4(r + e);
}
DEVINL float add1(float a, float b) { float r; asm("v_add_f32 %0, %1, %2" : "=v"(r) : "v"(a), "v"(b)); return r; }
DEVINL float mul1(float a, float b) { float r; asm("v_mul_f32 %0, %1, %2" : "=v"(r) : "v"(a), "v"(b)); return r; }
DEVINL v4f add4(v4f a, v4f b) { return (v4f){add1(a[0], b[0]), add1(a[1], b[1]), add1(a[2], b[2]), add1(a[3], b[3])}; }
DEVINL v4f mul4(v4f a, v4f b) { return (v4f){mul1(a[0], b[0]), mul1(a[1], b[1]), mul1(a[2], b[2]), mul1(a[3], b[3])}; }
#define PINV(x) asm volatile("" : "+v"(x))
#define PINS(x) asm volatile("" : "+s"(x))

DEVINL void set_flag(char* ws, unsigned idx, u64 tag, int nrep, int lane) {
  const __amdgpu_buffer_rsrc_t rs = __builtin_amdgcn_make_buffer_rsrc(ws, 0, WS_BYTES, 0x00020000);
  const unsigned lo = (unsigned)tag, hi = (unsigned)(tag >> 32);
  if (lane < 8 * nrep) __builtin_amdgcn_raw_buffer_store_b128((v4u){lo, hi, lo, hi}, rs, (lane >> 3) * (FL_N * 128) + (lane & 7) * 16, WS_FLAG + idx * 128, 16);
}
#define WO_GATE 16
#define NREP 8
#define YSLEEP 2
#define PRELOOK 0
#define HSL 2
#define NODOT 0
#define LATEOFF 0
#define PHINT 1
#define HEARLY 0
#define HSPLIT 2
#define RED_GATE 8
#define PUBSLEEP 1
#define PSLEEP 2
template <int SL = PSLEEP> DEVINL void wait_flags(char* ws, unsigned idx, int n, u64 tag, int lane) {
  if (lane < n) {
    GAS u64* p = (GAS u64*)(ws + WS_FLAG + (idx + lane) * 128u);
#pragma unroll 1
    for (unsigned spins = 0; spins < (1u << 16); ++spins) {
      if (__hip_atomic_load(p, RLX_AGENT) == tag) break;
      __builtin_amdgcn_s_sleep(SL);
    }
  }
  __builtin_amdgcn_fence(__ATOMIC_ACQUIRE, "wavefront");
  asm volatile("" ::: "memory");
}
DEVINL void arrive_flag(unsigned cnt, unsigned n, char* ws, unsigned idx, u64 tag, int lane) {
  unsigned old;
  asm volatile("s_waitcnt vmcnt(0)\n\ts_mov_b64 exec, 1\n\tds_add_rtn_u32 %0, %1, %2\n\ts_mov_b64 exec, -1\n\ts_waitcnt lgkmcnt(0)" : "=&v"(old) : "v"(cnt), "v"(1u) : "memory");
  if (__builtin_amdgcn_readfirstlane(old) == n - 1) set_flag(ws, idx, tag, NREP, lane);
}
DEVINL void arrive_hint(unsigned cnt, unsigned n, char* ws, unsigned idx, u64 tag, int lane) {
  unsigned old;
  asm volatile("s_mov_b64 exec, 1\n\tds_add_rtn_u32 %0, %1, %2\n\ts_mov_b64 exec, -1\n\ts_waitcnt lgkmcnt(0)" : "=&v"(old) : "v"(cnt), "v"(1u) : "memory");
  if (__builtin_amdgcn_readfirstlane(old) == n - 1) set_flag(ws, idx, tag, NREP, lane);
}
DEVINL void lds_arrive(unsigned cnt, int lane) {
  asm volatile("s_mov_b64 exec, 1\n\tds_add_u32 %0, %1\n\ts_mov_b64 exec, -1" :: "v"(cnt), "v"(1u) : "memory");
}
template <int SLEEP> DEVINL void lds_wait(unsigned cnt, unsigned n) {
  unsigned v, sv, spins;
  asm volatile("s_mov_b32 %2, 0x100000\n1:\n\tds_read_b32 %0, %3\n\ts_waitcnt lgkmcnt(0)\n\tv_readfirstlane_b32 %1, %0\n\ts_cmp_ge_u32 %1, %4\n\ts_cbranch_scc1 2f\n\t"
               "s_sleep %5\n\ts_sub_u32 %2, %2, 1\n\ts_cbranch_scc0 1b\n2:"
               : "=&v"(v), "=&s"(sv), "=&s"(spins) : "v"(cnt), "s"(n), "n"(SLEEP) : "memory", "scc");
}
#define ST(i)
#define STC(i)
#define STARG
#define STPASS

DEVINL void gemm_kv(v4f (&aK)[8], v4f (&aV)[8], v4f (&aT)[4], unsigned xa, unsigned xb, unsigned wa, unsigned wt STARG) {
  v4u xp[8], wp[3], xq[8], wq[3];
  unsigned so = L_KVSLOT;
#define RD(X, W, hs) do { X[0] = lds128((xa + so) ^ ((hs) * 64)); \
    W[0] = cat(ldtr(wa + so + (hs) * 5120), ldtr(wa + so + (hs) * 5120 + 2560)); \
    W[1] = cat(ldtr(wa + so + (hs) * 5120 + 10256), ldtr(wa + so + (hs) * 5120 + 2560 + 10256)); \
    W[2] = cat(ldtr(wt + so + (hs) * 5120), ldtr(wt + so + (hs) * 5120 + 2560)); \
    _Pragma("unroll") for (int m = 1; m < 8; ++m) X[m] = lds128((((m < 4 ? xa : xb) + so) ^ ((hs) * 64)) + (m & 3) * 2048); } while (0)
#define MM(X, W) do { _Pragma("unroll") for (int m = 0; m < 8; ++m) { aK[m] = mfma(X[m], W[0], aK[m]); aV[m] = mfma(X[m], W[1], aV[m]); if (m < 4) aT[m] = mfma(X[m], W[2], aT[m]); } } while (0)
#define MIX() do { _Pragma("unroll") for (int i = 0; i < 20; ++i) { __builtin_amdgcn_sched_group_barrier(0x8, 1, 0); __builtin_amdgcn_sched_group_barrier(0x100, 1, 0); } } while (0)
  bar(); SCHED();
  RD(xp, wp, 0);
  SCHED();
#pragma unroll 1
  for (int s = 0; s < 9; ++s) {
    RD(xq, wq, 1); MM(xp, wp); MIX(); SCHED();
    bar(); SCHED();
    so = (so == 0) ? 3 * L_KVSLOT : so - L_KVSLOT;
    RD(xp, wp, 0); MM(xq, wq); MIX(); SCHED();
  }
  RD(xq, wq, 1); MM(xp, wp); MIX(); SCHED();
  ST(1);
#pragma unroll
  for (int m = 0; m < 4; ++m) aT[m] = mfma(xq[m], wq[2], aT[m]);
#pragma unroll
  for (int m = 0; m < 8; ++m) aK[m] = mfma(xq[m], wq[0], aK[m]);
#pragma unroll
  for (int m = 0; m < 8; ++m) aV[m] = mfma(xq[m], wq[1], aV[m]);
#undef RD
#undef MM
#undef MIX
}
DEVINL void gemm_q(v4f (&acc)[2][5], unsigned xa, unsigned wa, char* ws, unsigned uidx, u64 tag, int lane) {
  v4u xp[2][2], wp[2][5], xq[2][2], wq[2][5];
  unsigned so = 0;
#define RD(X, W) do { _Pragma("unroll") for (int hs = 0; hs < 2; ++hs) { X[hs][0] = lds128((xa + so) ^ (hs * 64)); \
    _Pragma("unroll") for (int n = 0; n < 5; ++n) W[hs][n] = cat(ldtr(wa + so + hs * 5120 + n * 32), ldtr(wa + so + hs * 5120 + 2560 + n * 32)); \
    X[hs][1] = lds128(((xa + so) ^ (hs * 64)) + 2048); } so = (so == 3 * L_QSLOT) ? 0 : so + L_QSLOT; } while (0)
#define MM(X, W) do { _Pragma("unroll") for (int hs = 0; hs < 2; ++hs) _Pragma("unroll") for (int m = 0; m < 2; ++m) _Pragma("unroll") for (int n = 0; n < 5; ++n) \
    acc[m][n] = mfma(W[hs][n], X[hs][m], acc[m][n]); } while (0)
#define MIX() do { _Pragma("unroll") for (int i = 0; i < 20; ++i) { __builtin_amdgcn_sched_group_barrier(0x8, 1, 0); __builtin_amdgcn_sched_group_barrier(0x100, 1, 0); } \
    __builtin_amdgcn_sched_group_barrier(0x100, 4, 0); } while (0)
  arrive_flag(L_CNT + 4, 4, ws, uidx, tag, lane);
  bar(); SCHED();
  RD(xp, wp);
  SCHED();
#pragma unroll 1
  for (int s = 0; s < 4; ++s) {
    rawbar(); SCHED();
    RD(xq, wq); MM(xp, wp); MIX(); SCHED();
    rawbar(); SCHED();
    RD(xp, wp); MM(xq, wq); MIX(); SCHED();
  }
  rawbar(); SCHED();
  RD(xq, wq); MM(xp, wp); MIX(); SCHED();
  MM(xq, wq);
#undef RD
#undef MM
#undef MIX
}

struct ConvArgs { unsigned ha, ua, yo; v4u dg; };
DEVINL ConvArgs conv_args(unsigned cb, unsigned dbits, unsigned yoff, int lane) {
  const int i = lane & 15, g = lane >> 4, jt = 15 - i - 8 * g;
  ConvArgs c;
  c.ha = cb + (i >> 2) * L_CS + 16 * g + 8 * (i & 3) + 8;
  c.ua = cb + L_UOFF + 480 - 32 * i + 16 * g;
  c.yo = yoff + 16 * i + 256 * (g >> 1);
#pragma unroll
  for (int d = 0; d < 4; ++d) c.dg[d] = (jt == 2 * d) ? dbits : (jt == 2 * d + 1) ? (dbits << 16) : 0u;
  PINV(c.ha); PINV(c.ua); PINV(c.yo); PINV(c.dg);
  return c;
}
DEVINL v4u conv_pack(v4f a, v4f b) {
  v2u X = pk4(a), Y = pk4(b);
  auto r0 = __builtin_amdgcn_permlane16_swap(X.x, Y.x, false, false);
  auto r1 = __builtin_amdgcn_permlane16_swap(X.y, Y.y, false, false);
  return (v4u){r0[0], r1[0], r0[1], r1[1]};
}
DEVINL void conv_store(v4u o, int ta, unsigned yo, char* ws, int lane) {
  const __amdgpu_buffer_rsrc_t rs = __builtin_amdgcn_make_buffer_rsrc(ws, 0, WS_BYTES, 0x00020000);
  __builtin_amdgcn_raw_buffer_store_b128(o, rs, yo + ((lane & 16) ? 7 - ta : ta) * 512, 0, 16);
}
template <bool FULL>
DEVINL void conv_wave(v4f (&acc)[8], ConvArgs c, char* ws, int lane STARG) {
  unsigned ha = c.ha, ua = c.ua;
  v4u F[8], G[8], F2[8], G2[8];
#define CRD(F, G) do { F[0] = cat(ldtr(ha), ldtr(ha + 8)); _Pragma("unroll") for (int v = 0; v < 8; ++v) G[v] = lds128(ua + (7 - v) * 512); \
    _Pragma("unroll") for (int v = 1; v < 8; ++v) F[v] = cat(ldtr(ha + v * 512), ldtr(ha + v * 512 + 8)); ha += 64; ua += 64; PINV(ha); PINV(ua); } while (0)
#define CMM(F, G) do { _Pragma("unroll") for (int w = 0; w < 8; ++w) _Pragma("unroll") for (int t = w; t < 8; ++t) acc[t] = mfma(F[w], G[t - w], acc[t]); } while (0)
#define CMIX() do { _Pragma("unroll") for (int j = 0; j < 36; ++j) { __builtin_amdgcn_sched_group_barrier(0x8, 1, 0); __builtin_amdgcn_sched_group_barrier(0x100, 1, 0); } } while (0)
  CRD(F, G);
  SCHED();
#pragma unroll
  for (int t = 0; t < 8; ++t) acc[t] = mfma(c.dg, G[t], (v4f){0, 0, 0, 0});
  SCHED(); ST(16);
  if (FULL) {
#pragma unroll 1
    for (int it = 0; it < 3; ++it) {
      CRD(F2, G2); CMM(F, G); CMIX(); SCHED();
      CRD(F, G); CMM(F2, G2); CMIX(); SCHED();
    }
  }
  CRD(F2, G2); CMM(F, G); CMIX(); SCHED();
  ST(17);
  if (!FULL) { CMM(F2, G2); return; }
  bar(); SCHED();
#undef CRD
#undef CMM
#undef CMIX
#pragma unroll
  for (int a = 0; a < 4; ++a) {
    const int b = 7 - a;
#pragma unroll
    for (int w = 0; w <= b; ++w) { acc[b] = mfma(F2[w], G2[b - w], acc[b]); if (w <= a) acc[a] = mfma(F2[w], G2[a - w], acc[a]); }
  }
#pragma unroll
  for (int a = 0; a < 4; ++a) conv_store(conv_pack(acc[a], acc[7 - a]), a, c.yo, ws, lane);
  __builtin_amdgcn_sched_group_barrier(0x8, 12, 0);
#pragma unroll
  for (int a = 0; a < 3; ++a) {
#pragma unroll
    for (int j = 0; j < 8; ++j) { __builtin_amdgcn_sched_group_barrier(0x2, 1, 0); __builtin_amdgcn_sched_group_barrier(0x8, 1, 0); }
    __builtin_amdgcn_sched_group_barrier(0x40, 1, 0);
  }
}

__global__ void __launch_bounds__(512, 2) fused_kernel(Params p) {
  __shared__ __attribute__((aligned(1024))) char smem[163840];
  u64 pc; asm volatile("s_getpc_b64 %0" : "=s"(pc));
  const int tid = threadIdx.x, lane = tid & 63;
  const int b = blockIdx.x, xcd = b & 7, bi = b >> 3;
  warm(mk_rsrc((const void*)pc, CODE_BYTES), tid < 42 ? ((bi & 7) * 42 + tid) * 128 : OOB, 0);
  const int wv = __builtin_amdgcn_readfirstlane(tid >> 6);
  const bool isQ = bi >= 16;
  const int rt = 4 * (xcd >> 1) + (bi & 3), g = 4 * (xcd & 1) + ((bi >> 2) & 3);
  ST(0);
  typedef __attribute__((address_space(4))) const u64 cu64;
  typedef __attribute__((address_space(4))) const unsigned cu32;
  const int lw = wv & 3, iq = bi & 15;
  int k0 = ((xcd >> 1) * 5 >> 1) + 5 * (xcd & 1); k0 = k0 >= 10 ? k0 - 10 : k0;
  v4i rx, rw;
#define MK_XW() do { const u16 *x_ = p.x, *w_ = p.w; PINS(x_); PINS(w_); rx = mk_rsrc(x_, 2048 * 1280); rw = mk_rsrc(w_, 640 * 3840); } while (0)
  auto off_x = [&](int j) {
    int ka = k0 + 2 * j + (lane >> 5); ka = ka >= 10 ? ka - 10 : ka;
    unsigned vo = ((xcd >> 1) * 512 + iq * 32 + (lane & 31)) * 1280 + ka * 128; PINV(vo);
    return vo;
  };
  auto off_w = [&](int j) {
    const unsigned r10 = lane / 10, c10 = lane - r10 * 10, rho = 6 * j + r10; int kb = k0 + (rho >> 2); kb = kb >= 10 ? kb - 10 : kb;
    unsigned vo = (kb * 64 + iq * 4 + (rho & 3)) * 3840 + 1280 + (xcd & 1) * 640 + c10 * 128 + (c10 >= 5 ? 640 : 0); PINV(vo);
    vo = (lane < 60 && rho < 40) ? vo : OOB; PINV(vo);
    return vo;
  };
  auto warm_x = [&](int j) { warm(rx, off_x(j), 0); };
  auto warm_w = [&](int j) { warm(rw, off_w(j), 0); };
  unsigned xo0 = 0, xo1 = 0, wo_[5] = {0, 0, 0, 0, 0};
  auto loader_offsets = [&]() {
    const unsigned r = lane >> 3;
    xo0 = (rt * 128 + lw * 32 + r) * 1280 + (((lane & 7) ^ (r >> 1)) << 4);
    xo1 = (xo0 ^ 64) + 8 * 1280;
#pragma unroll
    for (int j = 0; j < 5; ++j) { const unsigned ch = j * 64 + lane, rho = ch / 10, c = ch - rho * 10;
      wo_[j] = (8 * ((rho >> 2) & 3) + 4 * (rho >> 4) + (rho & 3)) * 3840 + c * 16; PINV(wo_[j]); }
    PINV(xo0); PINV(xo1);
  };
  const unsigned kwcol = (640 + 640 * (lw & 1) + 80 * g) * 2 + (lw >> 1) * 32 * 3840, kwl = 16384 + (lw & 1) * 10256 + (lw >> 1) * 5120;
  auto issue_x = [&](int ks, unsigned so) {
#pragma unroll
    for (int j = 0; j < 4; ++j) dma(rx, (j & 1) ? xo1 : xo0, ks * 128 + (j >> 1) * 16 * 1280, so + lw * 4096 + j * 1024);
  };
  auto issue_kw = [&](int ks, unsigned so) {
#pragma unroll
    for (int j = 0; j < 5; ++j) dma(rw, wo_[j], kwcol + ks * 64 * 3840, so + kwl + j * 1024);
  };
  const int tt = 4 * xcd + (iq >> 2), cq = iq & 3, t0 = tt * 64;
  if (isQ) {
    const unsigned r20 = lane / 20, so = (t0 + wv * 8) * 1280, la = L_QKIMG + 2576 + wv * 2576;
    unsigned vo = r20 * 1280 + (lane - r20 * 20) * 16 + cq * 320, xv = off_x(1); PINV(vo);
    MK_XW();
    if (wv == 0) warm(rx, xv, 0);
#define KVDELAY 16
    if (KVDELAY) __builtin_amdgcn_s_sleep(KVDELAY);
    const v4i rk = mk_rsrc(p.k, 2048 * 1280), rv = mk_rsrc(p.v, 2048 * 1280);
    if (lane < 60) {
      dma(rk, vo, so, la); dma(rk, vo, so + 3840, la + 960); dma(rv, vo, so, la + 8 * 2576); dma(rv, vo, so + 3840, la + 8 * 2576 + 960);
      if (wv == 7) dma(rk, tt ? vo + (t0 - 3) * 1280 : OOB, 0, L_QKIMG + 1600);
      if (lane < 40) {
        dma(rk, vo, so + 7680, la + 1920); dma(rv, vo, so + 7680, la + 8 * 2576 + 1920);
        if (wv == 5 || wv == 6) dma(mk_rsrc(p.sk, 640 * 8), cq * 1280 + (wv - 5) * 640 + lane * 16, 0, L_SW + (wv - 5) * 640);
      }
    }
  } else if (wv >= 4) { loader_offsets(); MK_XW(); issue_x(k0, L_KVSLOT); issue_kw(k0, L_KVSLOT); }
  else { const unsigned xv = off_x(0), wz = off_w(wv - 1); MK_XW(); warm(wv == 0 ? rx : rw, wv == 0 ? xv : wv < 3 ? wz : OOB, 0);
    if (PHINT) __builtin_amdgcn_raw_buffer_store_b128((v4u){~0u, ~0u, ~0u, ~0u}, __builtin_amdgcn_make_buffer_rsrc(p.ws, 0, WS_BYTES, 0x00020000),
                                                      lane < 56 ? lane * 16 : OOB, WS_PART + (g * 16 + rt) * PART_STRIDE + wv * 896, 16);
  }
  ST(12);
  asm volatile("" ::: "memory");
  const int c0 = isQ ? 80 * g + 64 + rt : 80 * g + 4 * rt + lw;
  const v4i rws = mk_rsrc(p.ws, WS_BYTES), ro = mk_rsrc(p.wo, 640 * 1280);
  const __amdgpu_buffer_rsrc_t bws = __builtin_amdgcn_make_buffer_rsrc(p.ws, 0, WS_BYTES, 0x00020000);
  const unsigned rep = (xcd & (NREP - 1)) * FL_N;
  const int l15 = lane & 15, grp = lane >> 4;
  const unsigned trl = (4 * grp + (l15 >> 2)) * 160 + 8 * (lane & 3);
    const int gr0 = (4 * xcd + ((bi >> 2) & 3)) * 64, gct = (bi & 3) + 4 * (bi >> 4), rot = (bi & 1) + 2 * (bi >> 4);
  const __amdgpu_buffer_rsrc_t bh = __builtin_amdgcn_make_buffer_rsrc((void*)p.h, 0, 640 * 4096, 0x00020000);

  auto h_store = [&](unsigned cb, int j, v4u d, v2u m) {
    const unsigned a = cb + 32 + j * 1024 + lane * 16;
    const unsigned e0 = __builtin_amdgcn_alignbit(m.y, m.x, 16), e1 = __builtin_amdgcn_alignbit(d.x, m.y, 16), e2 = __builtin_amdgcn_alignbit(d.y, d.x, 16),
                   e3 = __builtin_amdgcn_alignbit(d.z, d.y, 16), e4 = __builtin_amdgcn_alignbit(d.w, d.z, 16);
    sts128(a, (v4u){e0, e1, e2, e3}); sts128(a + L_CS, (v4u){m.y, d.x, d.y, d.z}); sts128(a + 2 * L_CS, (v4u){e1, e2, e3, e4});
  };
  auto h_pads = [&](unsigned cb) {
    if (lane < 38) sts128(lane < 30 ? cb + L_UOFF + 4096 + lane * 16 : cb + ((lane - 30) >> 1) * L_CS + (lane & 1) * 16, (v4u){0, 0, 0, 0});
  };
  auto load_u = [&](int c, unsigned cb) {
#pragma unroll
    for (int j = 0; j < 4; ++j) dma_sc1(rws, lane * 16, WS_UT + c * 4096 + j * 1024, cb + L_UOFF + j * 1024);
  };
  unsigned vo5[5];
  auto row_offsets = [&]() {
#pragma unroll
    for (int j = 0; j < 5; ++j) { const unsigned ch = j * 64 + lane, r = ch / 10, c = ch - r * 10; vo5[j] = r * 1280 + c * 16; PINV(vo5[j]); }
  };
  auto load_q = [&](int i) {
    const v4i rq = mk_rsrc(p.q, 2048 * 1280);
    int gb = gct; PINS(gb);
#pragma unroll
    for (int j = 0; j < 5; ++j) dma(rq, vo5[j], (gr0 + i * 32) * 1280 + gb * 160, L_QT + i * 5120 + j * 1024);
  };
  auto load_wo = [&](int i0, int i1, int step) {
    int gb = gct; PINS(gb);
#pragma unroll 1
    for (int i = i0; i < i1; i += step)
#pragma unroll
      for (int j = 0; j < 5; ++j) dma(ro, vo5[j], i * 32 * 1280 + gb * 160, L_WO + i * 5120 + j * 1024);
  };
  unsigned oo3[3], stw;
  const unsigned sta = (wv < 4 ? L_ST : L_QT) + lw * 2560;
  auto out_offsets = [&]() {
#pragma unroll
    for (int j = 0; j < 3; ++j) { const unsigned ch = j * 64 + lane, r = ch / 10;
      oo3[j] = ch < 160 ? (gr0 + lw * 16 + r) * 2560 + (wv < 4 ? 1280 : 0) + gct * 160 + (ch - r * 10) * 16 : OOB; PINV(oo3[j]); }
    stw = sta + l15 * 160 + grp * 8; PINV(stw);
  };
  u64 tag; unsigned dbits;
#define GET_TAG() do { const char* w_ = p.ws; const u16* d_ = p.Dv; PINS(w_); PINS(d_); u64 e_ = *(cu64*)w_; dbits = ((cu32*)d_)[c0 >> 1]; PINS(e_); PINS(dbits); \
    dbits = (dbits >> (16 * (c0 & 1))) & 0xffffu; tag = e_ * 6364136223846793005ull + 1442695040888963407ull; } while (0)

  if (!isQ) {
    if (wv < 4) {
      if (tid == 0) { asm volatile("" :: "v"(smem)); sts128(L_CNT, (v4u){0, 0, 0, 0}); sts128(L_CNT + 16, (v4u){0, 0, 0, 0}); sts128(L_CNT + 32, (v4u){0, 0, 0, 0}); sts128(L_CNT + 48, (v4u){0, 0, 0, 0}); }
      GET_TAG();
      PINS(tag);
      const int hf = wv & 1;
      const bool isk = !(l15 & 4), own = (l15 >> 2) == 2 * hf + 1;
      const unsigned pc0 = L_PC + wv * 896, trash = L_DUMMY + 256;
      const int jb = own ? (l15 & 3) : l15 == 0 ? 4 : -1;
      unsigned pb = jb >= 0 ? pc0 + (80 + 5 * grp + jb) * 8 : trash, pcc = grp == 2 * hf ? pc0 + (64 + l15) * 8 : trash,
               pd = (grp == 2 * hf && jb >= 0) ? pc0 + (100 + jb) * 8 : trash;
      PINV(pb); PINV(pcc); PINV(pd);
      const ConvArgs ca = conv_args(L_KVCONV + wv * L_CH, dbits, WS_YT + c0 * 4096, lane);
      out_offsets();
      if (lane < 7) sts64(pc0 + (105 + lane) * 8, (v2u){0, 0});
      v4f aK[8], aV[8], aT[4];
#pragma unroll
      for (int m = 0; m < 8; ++m) { aK[m] = (v4f){1e-6f, 1e-6f, 1e-6f, 1e-6f}; aV[m] = (v4f){0, 0, 0, 0}; }
#pragma unroll
      for (int m = 0; m < 4; ++m) { const float c = isk ? 1e-6f : 0.f; aT[m] = (v4f){c, c, c, c}; }
      const unsigned x0 = l15 * 128 + ((grp ^ (l15 >> 1)) << 4), trr = (4 * grp + (l15 >> 2)) * 160;
      gemm_kv(aK, aV, aT, x0 + hf * 8192, x0 + (1 - hf) * 8192, 16384 + trl + wv * 40, 16384 + trr + (lane & 1) * 10256 + (wv >> 1) * 80 + (lane & 2) * 20 + 32 STPASS);
      v4u tf[4], kf[4], vf[4];
#pragma unroll
      for (int t = 0; t < 2; ++t) {
        const v4u a = cat(phi4(aT[2 * t]), phi4(aT[2 * t + 1])), c = cat(pk4(aT[2 * t]), pk4(aT[2 * t + 1]));
#pragma unroll
        for (int d = 0; d < 4; ++d) tf[t][d] = isk ? a[d] : c[d];
        sts128(L_KVSLOT + wv * 2048 + t * 1024 + lane * 16, tf[t]);
      }
      asm volatile("" ::: "memory");
      sts32(L_CNT + 32 + wv * 4, 1u);
      v4f dA = {0, 0, 0, 0}, dB = dA, dC = dA, dD = dA;
      auto kvstep = [&](int t) {
        v4u tb;
#pragma unroll
        for (int d = 0; d < 4; ++d) tb[d] = own ? tf[t][d] : 0x3f803f80u;
        dA = mfma(kf[t], vf[t], dA); dB = mfma(kf[t], tb, dB); dC = mfma(tf[t], vf[t], dC); dD = mfma(tf[t], tb, dD);
      };
#pragma unroll
      for (int t = 0; t < 3; ++t) { kf[t] = cat(phi4(aK[2 * t]), phi4(aK[2 * t + 1])); vf[t] = cat(pk4(aV[2 * t]), pk4(aV[2 * t + 1])); }
      kvstep(0);
      PINV(kf[2]); PINV(vf[2]);
      asm volatile("" ::: "memory");
      unsigned mark = lds32(L_CNT + 32 + (wv ^ 1) * 4);
      asm volatile("" ::: "memory");
      tf[2] = lds128(L_KVSLOT + (wv ^ 1) * 2048 + lane * 16); tf[3] = lds128(L_KVSLOT + (wv ^ 1) * 2048 + 1024 + lane * 16);
      asm volatile("" ::: "memory");
      kf[3] = cat(phi4(aK[6]), phi4(aK[7])); vf[3] = cat(pk4(aV[6]), pk4(aV[7]));
      kvstep(1);
      PINV(kf[3]); PINV(vf[3]);
      if (__builtin_amdgcn_readfirstlane(mark) == 0) {
        lds_wait<0>(L_CNT + 32 + (wv ^ 1) * 4, 1);
        tf[2] = lds128(L_KVSLOT + (wv ^ 1) * 2048 + lane * 16); tf[3] = lds128(L_KVSLOT + (wv ^ 1) * 2048 + 1024 + lane * 16);
      }
      kvstep(2); kvstep(3);
      sts64(pc0 + lane * 8, pk4(dA)); sts64(pb, pk4(dB)); sts64(pcc, pk4(dC)); sts64(pd, pk4(dD));
      waitv<0>();
      sts32(L_CNT + 32 + wv * 4, 2u);
      ST(2);
      lds_wait<0>(L_CNT + 48 + wv * 4, 1);
      ST(4);
      v4f acc[8];
      conv_wave<true>(acc, ca, p.ws, lane STPASS);
      ST(5);
    } else {
      auto piece = [&](int q) {
        const int st = q / 9, j = q % 9; const int ks = k0 + st >= 10 ? k0 + st - 10 : k0 + st; const unsigned so = ((st & 3) ^ 1) * L_KVSLOT;
        if (j < 4) dma(rx, (j & 1) ? xo1 : xo0, ks * 128 + (j >> 1) * 16 * 1280, so + lw * 4096 + j * 1024);
        else dma(rw, wo_[j - 4], kwcol + ks * 64 * 3840, so + kwl + (j - 4) * 1024);
      };
      GET_TAG();
      row_offsets();
      out_offsets();
      unsigned uw = lane < 8 ? (lw * 8 + lane) * 128 : OOB; PINV(uw);
      unsigned wov = lane < 50 ? (bi * 200 + lw * 50 + lane) * 128 : OOB; PINV(wov);
      unsigned pso = lane < 56 ? WS_PART + (g * 16 + rt) * PART_STRIDE + lw * 896 + lane * 16 : OOB; PINV(pso);
#define KVSTEP(s, q0, q1) do { _Pragma("unroll") for (int q = (q0); q < (q1); ++q) piece(q); waitv<(q1) - 9 * ((s) + 1)>(); rawbar(); } while (0)
      KVSTEP(0, 9, 13); ST(14);
      KVSTEP(1, 13, 23); KVSTEP(2, 23, 33); KVSTEP(3, 33, 43); KVSTEP(4, 43, 53);
      KVSTEP(5, 53, 63); KVSTEP(6, 63, 73); KVSTEP(7, 73, 83); KVSTEP(8, 83, 90);
#undef KVSTEP
      ST(15);
      const unsigned cb = L_KVCONV + lw * L_CH + 32;
      {
        const v4i rhs = mk_rsrc((const char*)p.h - 16, 640 * 4096 + 16);
#pragma unroll
        for (int j = 0; j < 4; ++j) dma(rhs, lane * 16, c0 * 4096 + j * 1024 + 16, cb + 3 * L_CS + j * 1024);
#pragma unroll
        for (int j = 1; j < 4; ++j) dma(rhs, lane * 16, c0 * 4096 + j * 1024 + 12, cb + L_CS + j * 1024);
      }
      h_pads(L_KVCONV + lw * L_CH);
      waitv<7>(); rawbar();
      ST(13);
      __builtin_amdgcn_s_setprio(3);
      warm(ro, wov, 3200 * 128);
      wait_flags(p.ws, rep + FL_U + (g >> 1) * 32 + lw * 8, 8, tag, lane);
      waitv<0>();
      lds_arrive(L_CNT + 28, lane);
      v4u hd[4]; v2u hm[4];
#pragma unroll
      for (int j = 0; j < 4; ++j) { hd[j] = lds128(cb + 3 * L_CS + j * 1024 + lane * 16); hm[j] = lds64(cb + 3 * L_CS + j * 1024 + lane * 16 - 8); }
      lds_wait<0>(L_CNT + 28, 4);
      load_u(c0, L_KVCONV + lw * L_CH);
      warm_sc1(rws, uw, WS_UT + (80 * g + 64 + rt) * 4096);
      warm(ro, wov, 0);
      auto h_odd = [&](int j0, int j1) {
#pragma unroll
        for (int j = j0; j < j1; ++j) {
          const unsigned a = cb + j * 1024 + lane * 16; const v4u d = hd[j]; const v2u m = hm[j];
          const unsigned e0 = __builtin_amdgcn_alignbit(m.y, m.x, 16), e1 = __builtin_amdgcn_alignbit(d.x, m.y, 16), e2 = __builtin_amdgcn_alignbit(d.y, d.x, 16),
                         e3 = __builtin_amdgcn_alignbit(d.z, d.y, 16), e4 = __builtin_amdgcn_alignbit(d.w, d.z, 16);
          sts128(a, (v4u){e0, e1, e2, e3}); sts128(a + 2 * L_CS, (v4u){e1, e2, e3, e4});
          if (j == 0) sts128(a + L_CS, (v4u){m.y, d.x, d.y, d.z});
        }
      };
      h_odd(0, HEARLY);
      ST(1);
      lds_wait<PUBSLEEP>(L_CNT + 32 + lw * 4, 2);
      __builtin_amdgcn_raw_buffer_store_b128(lds128(L_PC + lw * 896 + lane * 16), bws, pso, 0, 16);
      if (PHINT) arrive_hint(L_CNT + 12, 4, p.ws, FL_P + g * 16 + rt, tag, lane);
      ST(5);
      h_odd(HEARLY, 4);
      waitv<3>();
      sts32(L_CNT + 48 + lw * 4, 1u);
      ST(16);
      arrive_flag(L_CNT + 8, 4, p.ws, (PHINT ? FL_P2 : FL_P) + g * 16 + rt, tag, lane);
      ST(12);
      lds_wait<1>(L_CNT + 8, 4);
      if (lw < 2) load_q(lw);
      else warm(mk_rsrc(p.q, 2048 * 1280), (gr0 + (lw - 2) * 32 + (lane >> 1)) * 1280 + (gct + 4) * 160 + (lane & 1) * 156, 0);
      v4u wr[10];
      {
        const __amdgpu_buffer_rsrc_t bo = __builtin_amdgcn_make_buffer_rsrc((void*)p.wo, 0, 640 * 1280, 0x00020000);
#pragma unroll
        for (int j = 0; j < 10; ++j) wr[j] = __builtin_amdgcn_raw_buffer_load_b128(bo, vo5[j % 5], (12 + lw + (j / 5) * 4) * 32 * 1280 + gct * 160, 0);
      }
      SCHED();
      load_wo(lw, 12, 4);
      waitv<0>();
      ST(17);
      bar();
#pragma unroll
      for (int j = 0; j < 10; ++j) sts128(L_WO + (12 + lw + (j / 5) * 4) * 5120 + (j % 5) * 1024 + lane * 16, wr[j]);
      __builtin_amdgcn_s_setprio(0);
      lds_arrive(L_CNT + 20, lane);
    }
  } else {
    if (tid == 0) { asm volatile("" :: "v"(smem)); sts128(L_CNT, (v4u){0, 0, 0, 0}); sts128(L_CNT + 16, (v4u){0, 0, 0, 0}); sts128(L_CNT + 32, (v4u){0, 0, 0, 0}); }
    if (wv < 4) {
      GET_TAG();
      out_offsets();
      waitv<0>(); bar();
      ST(1);
      {
        const int tg = lane & 7;
        unsigned ka = L_QKIMG + tg * 2576 + (8 * wv + (lane >> 3)) * 4, sa = L_SW + (8 * wv + (lane >> 3)) * 16;
        unsigned uo = WS_UT + (160 * cq + 16 * wv + 2 * (lane >> 3)) * 4096 + 2 * (2040 - t0 - 8 * tg);
        unsigned kr[11], vr[8]; v4u sw;
#define PRD() do { PINV(ka); sw = lds128(sa); _Pragma("unroll") for (int j = 0; j < 11; ++j) kr[j] = lds32(ka + (j < 3 ? (5 + j) * 320 : 2576 + (j - 3) * 320)); \
          _Pragma("unroll") for (int j = 0; j < 8; ++j) vr[j] = lds32(ka + (L_QVIMG - L_QKIMG) + j * 320); ka += 128; sa += 512; } while (0)
        PRD();
        v4u ua[3], ub[3];
#pragma unroll
        for (int it = 0; it < 3; ++it) {
          if (it == 2 && wv >= 2) break;
          v2f kk[11], vv[8];
#pragma unroll
          for (int j = 0; j < 11; ++j) kk[j] = (v2f){blo(kr[j]), bhi(kr[j])};
#pragma unroll
          for (int j = 0; j < 8; ++j) vv[j] = (v2f){blo(vr[j]), bhi(vr[j])};
          const v2f w0 = {blo(sw.x), blo(sw.z)}, w1 = {bhi(sw.x), bhi(sw.z)}, w2 = {blo(sw.y), blo(sw.w)}, w3 = {bhi(sw.y), bhi(sw.w)};
          SCHED();
          if (it < 2) PRD();
          SCHED();
          v2f uu[8];
#pragma unroll
          for (int j = 0; j < 8; ++j) uu[j] = (kk[j] * w3 + kk[j + 1] * w2 + kk[j + 2] * w1 + kk[j + 3] * w0) * vv[j];
          ua[it] = (v4u){pk2(uu[7].x, uu[6].x), pk2(uu[5].x, uu[4].x), pk2(uu[3].x, uu[2].x), pk2(uu[1].x, uu[0].x)};
          ub[it] = (v4u){pk2(uu[7].y, uu[6].y), pk2(uu[5].y, uu[4].y), pk2(uu[3].y, uu[2].y), pk2(uu[1].y, uu[0].y)};
          PINV(ua[it]); PINV(ub[it]);
          if (it == 2) { __builtin_amdgcn_raw_buffer_store_b128(ua[2], bws, uo, 128 * 4096, 16); __builtin_amdgcn_raw_buffer_store_b128(ub[2], bws, uo, 129 * 4096, 16); }
        }
        ST(13);
#pragma unroll
        for (int it = 0; it < 2; ++it) { __builtin_amdgcn_raw_buffer_store_b128(ua[it], bws, uo, it * 64 * 4096, 16); __builtin_amdgcn_raw_buffer_store_b128(ub[it], bws, uo, (it * 64 + 1) * 4096, 16); }
#undef PRD
      }
      ST(2);
      unsigned ka[13], za[5];
      {
        int n = 0;
#pragma unroll
        for (int a = 0; a < 5; ++a) {
          const int Dq = 4 * a + grp, hh = Dq / 5, dq = Dq - 5 * hh, eb = l15 - 20 * hh;
          za[a] = L_KVC + ((hh == (l15 & 3)) ? hh * 112 + 84 + 5 * dq : 448) * 8; PINV(za[a]);
#pragma unroll
          for (int bb = 0; bb < 5; ++bb) if (bb >= a - 1 && bb <= a + 1) { const int e = 16 * bb + eb; ka[n] = L_KVC + (((unsigned)e < 16u) ? hh * 112 + 16 * dq + e : ((unsigned)e < 20u) ? hh * 112 + 64 + 5 * dq + e : 448) * 8; PINV(ka[n]); ++n; }
        }
      }
      unsigned po = (tid < 224) ? WS_PART + g * 16 * PART_STRIDE + tid * 16 : OOB; PINV(po);
      v4f acc[2][5];
#pragma unroll
      for (int m = 0; m < 2; ++m)
#pragma unroll
        for (int n = 0; n < 5; ++n) acc[m][n] = (v4f){1e-6f, 1e-6f, 1e-6f, 1e-6f};
      const unsigned xa = (wv * 32 + l15) * 128 + ((grp ^ (l15 >> 1)) << 4);
      gemm_q(acc, xa, 16384 + trl, p.ws, FL_U + cq * 32 + tt, tag, lane);
      __builtin_amdgcn_s_setprio(2);
      ST(3);
      v2u f0 = {0, 0};
      if (PRELOOK) f0 = __builtin_amdgcn_raw_buffer_load_b64(bws, lane < 16 ? lane * 128 : OOB, WS_FLAG + (rep + FL_P + g * 16) * 128, 16);
      SCHED();
      v2u pq[2][5];
#pragma unroll
      for (int m = 0; m < 2; ++m)
#pragma unroll
        for (int n = 0; n < 5; ++n) { pq[m][n] = phi4(acc[m][n]); PINV(pq[m][n]); }
      lds_arrive(L_CNT + 28, lane);
      ST(4);
      PINV(f0);
      if (!PRELOOK || !__all(lane >= 16 || (f0.x == (unsigned)tag && f0.y == (unsigned)(tag >> 32)))) wait_flags(p.ws, rep + FL_P + g * 16, 16, tag, lane);
      asm volatile("" ::: "memory");
      ST(6);
#pragma unroll 1
      for (int pass = 0; pass < 2; ++pass) {
        v4u r[16];
#pragma unroll
        for (int j = 0; j < 16; ++j) { r[j] = __builtin_amdgcn_raw_buffer_load_b128(bws, po, ((j + 4 * bi) & 15) * PART_STRIDE, 16); SCHED(); }
        if (pass == 0) lds_arrive(L_CNT + 32, lane);
        v4f s0 = {0, 0, 0, 0}, s1 = s0;
        unsigned sel0 = 0x00003f80u, sel1 = 0x3f800000u; PINV(sel0); PINV(sel1);
        auto dot = [](unsigned x, unsigned sel, float c) { return __builtin_amdgcn_fdot2_f32_bf16(__builtin_bit_cast(v2bf, x), __builtin_bit_cast(v2bf, sel), c, false); };
#pragma unroll
        for (int j = 0; j < 16; ++j) if (NODOT) { s0 = add4(s0, (v4f){blo(r[j].x), bhi(r[j].x), blo(r[j].y), bhi(r[j].y)}); s1 = add4(s1, (v4f){blo(r[j].z), bhi(r[j].z), blo(r[j].w), bhi(r[j].w)}); } else { s0[0] = dot(r[j].x, sel0, s0[0]); s0[1] = dot(r[j].x, sel1, s0[1]); s0[2] = dot(r[j].y, sel0, s0[2]); s0[3] = dot(r[j].y, sel1, s0[3]);
          s1[0] = dot(r[j].z, sel0, s1[0]); s1[1] = dot(r[j].z, sel1, s1[1]); s1[2] = dot(r[j].w, sel0, s1[2]); s1[3] = dot(r[j].w, sel1, s1[3]); }
        sts128(L_KVC + tid * 16, cat(pk4(s0), pk4(s1)));
        if (!PHINT) break;
        const float chk = (s0[0] + s0[2]) + (s1[0] + s1[2]);
        if (!__any(chk != chk)) break;
        wait_flags(p.ws, rep + FL_P2 + g * 16, 16, tag, lane);
      }
      ST(17);
      lds_arrive(L_CNT + 8, lane); lds_wait<0>(L_CNT + 8, 4);
      ST(7);
      {
        const v2u z2 = {0, 0};
        const v4u z0 = cat(lds64(za[0]), lds64(za[1])), z1 = cat(lds64(za[2]), lds64(za[3])), zz = cat(lds64(za[4]), z2);
        const v4u a0 = cat(lds64(ka[0]), lds64(ka[2])), a1 = cat(lds64(ka[1]), lds64(ka[3])), b1 = cat(lds64(ka[5]), z2), a2 = cat(z2, lds64(ka[4])), b2 = cat(lds64(ka[6]), lds64(ka[8])),
                  b3 = cat(lds64(ka[7]), lds64(ka[9])), c3 = cat(lds64(ka[11]), z2), b4 = cat(z2, lds64(ka[10])), c4 = cat(lds64(ka[12]), z2);
        const v4f zero = {0, 0, 0, 0};
#pragma unroll
        for (int m = 0; m < 2; ++m) {
          const v4u s0 = cat(pq[m][0], pq[m][1]), s1 = cat(pq[m][2], pq[m][3]), s2 = cat(pq[m][4], z2);
          v4f den = mfma(z0, s0, zero); den = mfma(z1, s1, den); den = mfma(zz, s2, den);
          v4f n0 = mfma(a0, s0, zero);
          v4f n1 = mfma(a1, s0, zero); n1 = mfma(b1, s1, n1);
          v4f n2 = mfma(a2, s0, zero); n2 = mfma(b2, s1, n2);
          v4f n3 = mfma(b3, s1, zero); n3 = mfma(c3, s2, n3);
          v4f n4 = mfma(b4, s1, zero); n4 = mfma(c4, s2, n4);
          const float r0 = __builtin_amdgcn_rcpf(den[0] + 1e-6f), r1 = __builtin_amdgcn_rcpf(den[1] + 1e-6f), r2 = __builtin_amdgcn_rcpf(den[2] + 1e-6f), r3 = __builtin_amdgcn_rcpf(den[3] + 1e-6f);
          n0 *= r0; n1 *= (grp < 1) ? r0 : r1; n2 *= (grp < 2) ? r1 : r2; n3 *= (grp < 3) ? r2 : r3; n4 *= r3;
          const unsigned oo = WS_OF + ((rt * 8 + 2 * wv + m) * 8 + g) * 2560;
          __builtin_amdgcn_raw_buffer_store_b128(cat(pk4(n0), pk4(n1)), bws, lane * 16, oo, 16);
          __builtin_amdgcn_raw_buffer_store_b128(cat(pk4(n2), pk4(n3)), bws, lane * 16 + 1024, oo, 16);
          __builtin_amdgcn_raw_buffer_store_b64(pk4(n4), bws, lane * 8 + 2048, oo, 16);
        }
        ST(8);
        waitv<0>();
        set_flag(p.ws, FL_O + (rt * 4 + wv) * 8 + g, tag, 1, lane);
        __builtin_amdgcn_s_setprio(0);
      }
      lds_wait<0>(L_CNT + 12, 4);
    } else {
      if (lw == 2) warm_x(2); else warm_w(2 + lw - (lw >> 1));
      GET_TAG();
      loader_offsets();
      unsigned w1o = (lw & 1) ? off_w(5 + (lw >> 1)) : off_x(3 + (lw >> 1)), wqo;
      { const unsigned r5 = lane / 5, rho = 12 * lw + r5; int kb = k0 + (rho >> 2); kb = kb >= 10 ? kb - 10 : kb;
        wqo = (lane < 60 && rho < 40) ? (kb * 64 + iq * 4 + (rho & 3)) * 3840 + (xcd & 1) * 640 + (lane - r5 * 5) * 128 : OOB; PINV(wqo); }
      waitv<1>(); bar();
      warm((lw & 1) ? rw : rx, w1o, 0);
      warm(rw, wqo, 0);
      dma(mk_rsrc(p.h, 640 * 4096), lane * 16, c0 * 4096 + lw * 1024, L_QCONV + 32 + 3 * L_CS + lw * 1024);
      ST(14);
      const unsigned wcol = 160 * g + (lw >> 1) * 32 * 3840, wl = 16384 + (lw >> 1) * 5120;
      const unsigned w0 = (lw & 1) ? wo_[1] : wo_[0], w1 = (lw & 1) ? wo_[3] : wo_[2];
      auto issue_qw = [&](int ks, unsigned so) {
        dma(rw, w0, wcol + ks * 64 * 3840, so + wl + (lw & 1) * 1024);
        dma(rw, w1, wcol + ks * 64 * 3840, so + wl + (lw & 1) * 1024 + 2048);
        if (!(lw & 1)) dma(rw, wo_[4], wcol + ks * 64 * 3840, so + wl + 4096);
      };
      int ks = k0 == 9 ? 0 : k0 + 1;
      issue_x(k0, 0); issue_qw(k0, 0);
      row_offsets();
      if (wv == 4) h_pads(L_QCONV);
      const ConvArgs ca = conv_args(L_QCONV + 128 * lw, lw ? 0u : dbits, WS_YT + c0 * 4096, lane);
      issue_qw(ks, L_QSLOT);
      if (lw & 1) waitv<2>(); else waitv<3>();
      bar();
      __builtin_amdgcn_s_setprio(1);
      ST(13);
      issue_x(ks, L_QSLOT);
      unsigned so = 2 * L_QSLOT;
#pragma unroll 1
      for (int s = 1; s < 9; ++s) {
        ks = ks == 9 ? 0 : ks + 1;
        issue_x(ks, so); issue_qw(ks, so);
        so = (so == 3 * L_QSLOT) ? 0 : so + L_QSLOT;
        if (lw & 1) waitv<6>(); else waitv<7>();
        rawbar();
        if (s == 1) ST(2);
        if (s == 4) ST(3);
      }
      if (lw < 3) { load_wo(10 + lw, 13, 4); waitv<5>(); } else waitv<0>();
      rawbar();
      if (lw == 3) { wait_flags(p.ws, rep + FL_U + (g >> 1) * 32, 32, tag, lane); load_u(c0, L_QCONV); }
      { const unsigned a = L_QCONV + 32 + 3 * L_CS + lw * 1024 + lane * 16; h_store(L_QCONV, lw, lds128(a), lds64(a - 8)); }
      if (lw == 3) waitv<0>(); else load_wo(13 + lw, 20, 3);
      lds_arrive(L_CNT + 28, lane);
      __builtin_amdgcn_s_setprio(0);
      lds_wait<HSL>(L_CNT + 28, 8);
      ST(15);
      v4f acc[8];
      conv_wave<false>(acc, ca, p.ws, lane STPASS);
      waitv<0>();
      lds_arrive(L_CNT + 12, lane);
      ST(5);
      lds_wait<1>(L_CNT + 32, 4);
      load_wo(lw, 10, 4);
      if (lw >= 2) load_q(lw - 2);
      ST(6);
      lds_wait<0>(L_CNT + 12, 4);
#pragma unroll
      for (int t = 0; t < 8; ++t) sts128(L_YT + (t * 4 + lw) * 1024 + lane * 16, __builtin_bit_cast(v4u, acc[t]));
      waitv<0>();
      lds_arrive(L_CNT + 20, lane); lds_wait<0>(L_CNT + 20, 4);
      v4f ya = __builtin_bit_cast(v4f, lds128(L_YT + lw * 4096 + lane * 16)), yb = __builtin_bit_cast(v4f, lds128(L_YT + (7 - lw) * 4096 + lane * 16));
#pragma unroll
      for (int w = 1; w < 4; ++w) { ya = add4(ya, __builtin_bit_cast(v4f, lds128(L_YT + (lw * 4 + w) * 1024 + lane * 16))); yb = add4(yb, __builtin_bit_cast(v4f, lds128(L_YT + ((7 - lw) * 4 + w) * 1024 + lane * 16))); }
      conv_store(conv_pack(ya, yb), lw, ca.yo, p.ws, lane);
      ST(7);
      out_offsets();
      arrive_flag(L_CNT, 4, p.ws, FL_Y + g * 32 + 16 + rt, tag, lane);
      lds_wait<1>(L_CNT, 4);
    }
  }
  ST(9);
#define AUX_B 0
#define AUX_A 0
  const __amdgpu_buffer_rsrc_t bout = __builtin_amdgcn_make_buffer_rsrc(p.out, 0, 2048 * 2560, 0x00020000);
  v2u res[5];
  if (wv < 4) {
    const int mt = (gr0 >> 4) + wv;
    unsigned wbq[4];
#pragma unroll
    for (int y = 0; y < 4; ++y) { wbq[y] = L_WO + trl + (y ^ rot) * 25600; PINV(wbq[y]); }
    v4u wf[7][5];
#define WRD(t, n) do { const int y_ = (t) / 5, s_ = (t) % 5; const unsigned ra = (s_ == 4) ? 64 : (s_ >> 1) * 80 + (s_ & 1) * 32, rb = (s_ == 4) ? 144 : ra + 16; \
      wf[(t) % 7][n] = cat(ldtr(wbq[y_] + ra * 160 + (n) * 32), ldtr(wbq[y_] + rb * 160 + (n) * 32)); } while (0)
#pragma unroll
    for (int f = 0; f < 35; ++f) WRD(f / 5, f % 5);
    SCHED();
    ST(14);
    if (!isQ) arrive_flag(L_CNT + 4, 4, p.ws, FL_Y + g * 32 + rt, tag, lane);
    ST(15);
    lds_wait<1>(L_CNT + 20, 4);
    if (b == 0 && wv == 0) { wait_flags(p.ws, FL_U, 64, tag, lane); wait_flags(p.ws, FL_U + 64, 64, tag, lane); }
    wait_flags(p.ws, FL_O + (mt >> 1) * 8, 8, tag, lane);
    ST(10);
    v4u of[20];
#define ORD(t) do { const int s_ = (t) % 5; const unsigned oo = WS_OF + (mt * 8 + 2 * (((t) / 5) ^ rot)) * 2560; \
      if (s_ < 4) of[t] = __builtin_amdgcn_raw_buffer_load_b128(bws, lane * 16 + (s_ & 1) * 1024, oo + (s_ >> 1) * 2560, 16); \
      else of[t] = cat(__builtin_amdgcn_raw_buffer_load_b64(bws, lane * 8 + 2048, oo, 16), __builtin_amdgcn_raw_buffer_load_b64(bws, lane * 8 + 2048, oo + 2560, 16)); } while (0)
#define OAHEAD 12
#pragma unroll
    for (int t = 0; t < OAHEAD; ++t) { ORD(t); SCHED(); }
    if (b == 0 && tid == 0) __hip_atomic_store((GAS u64*)p.ws, tag, RLX_AGENT);
    SCHED();
    v4f acc[5];
#pragma unroll
    for (int n = 0; n < 5; ++n) acc[n] = (v4f){0, 0, 0, 0};
#pragma unroll
    for (int t = 0; t < 20; ++t) {
      v4u cur[5];
#pragma unroll
      for (int n = 0; n < 5; ++n) cur[n] = wf[t % 7][n];
#pragma unroll
      for (int f = 0; f < 65; ++f) if (t >= 1 && f >= (t - 1) * 15 / 4 && f < t * 15 / 4) WRD(7 + f / 5, f % 5);
#pragma unroll
      for (int n = 0; n < 5; ++n) acc[n] = mfma(cur[n], of[t], acc[n]);
      if (t + OAHEAD < 20) ORD(t + OAHEAD);
      __builtin_amdgcn_sched_group_barrier(0x8, 1, 0); __builtin_amdgcn_sched_group_barrier(0x100, 2, 0);
      __builtin_amdgcn_sched_group_barrier(0x8, 1, 0); __builtin_amdgcn_sched_group_barrier(0x100, 1, 0); __builtin_amdgcn_sched_group_barrier(0x20, 1, 0);
      __builtin_amdgcn_sched_group_barrier(0x8, 1, 0); __builtin_amdgcn_sched_group_barrier(0x100, 2, 0);
      __builtin_amdgcn_sched_group_barrier(0x8, 1, 0); __builtin_amdgcn_sched_group_barrier(0x100, 1, 0); __builtin_amdgcn_sched_group_barrier(0x20, 1, 0);
      __builtin_amdgcn_sched_group_barrier(0x8, 1, 0); __builtin_amdgcn_sched_group_barrier(0x100, 2, 0);
      SCHED();
    }
#undef WRD
#undef ORD
#pragma unroll
    for (int n = 0; n < 5; ++n) res[n] = pk4(acc[n]);
  } else {
    v4f qf[5];
#pragma unroll
    for (int n = 0; n < 5; ++n) { const v2u u = lds64(stw + n * 32); qf[n] = (v4f){blo(u.x), bhi(u.x), blo(u.y), bhi(u.y)}; PINV(qf[n]); }
    unsigned yo = (lane >> 3) * 4096 + (lane & 3) * 16 + (lane & 4) * 64; PINV(yo);
    unsigned ya = L_YT + (4 * grp + (l15 >> 2)) * 128 + (lane & 2) * 32 + lw * 16 + (lane & 1) * 8; PINV(ya);
    const unsigned ys = WS_YT + (80 * gct + 20 * lw) * 4096 + (gr0 >> 8) * 512 + (gr0 & 192);
    ST(4);
    wait_flags<YSLEEP>(p.ws, rep + FL_Y + gct * 32 + lw * 5, lw == 3 ? 17 : 5, tag, lane);
    ST(10);
    dma_sc1(rws, yo, ys, L_YT + lw * 2560); dma_sc1(rws, yo, ys + 8 * 4096, L_YT + lw * 2560 + 1024);
    if (lane < 32) dma_sc1(rws, yo, ys + 16 * 4096, L_YT + lw * 2560 + 2048);
    waitv<0>();
    lds_arrive(L_CNT + 24, lane); lds_wait<1>(L_CNT + 24, 4);
#pragma unroll
    for (int n = 0; n < 5; ++n) { const v2u u = ldtr(ya + n * 2048); res[n] = pk4(mul4(qf[n], (v4f){blo(u.x), bhi(u.x), blo(u.y), bhi(u.y)})); }
  }
#pragma unroll
  for (int n = 0; n < 5; ++n) sts64(stw + n * 32, res[n]);
  asm volatile("" ::: "memory");
#pragma unroll
  for (int j = 0; j < 3; ++j) __builtin_amdgcn_raw_buffer_store_b128(lds128(sta + j * 1024 + lane * 16), bout, oo3[j], 0, AUX_B);
  ST(11);
}

extern "C" void kernel_launch(void* const* d_in, const int* in_sizes, int n_in, void* d_out, int out_size, void* d_ws, size_t ws_size, hipStream_t stream) {
  Params p;
  p.q = (const u16*)d_in[0]; p.k = (const u16*)d_in[1]; p.v = (const u16*)d_in[2]; p.sk = (const u16*)d_in[3]; p.h = (const u16*)d_in[4];
  p.Dv = (const u16*)d_in[5]; p.x = (const u16*)d_in[6]; p.w = (const u16*)d_in[7]; p.wo = (const u16*)d_in[8];
  p.out = (u16*)d_out; p.ws = (char*)d_ws;
  fused_kernel<<<256, 512, 0, stream>>>(p);
}
```

```cpp
#include <hip/hip_runtime.h>
typedef unsigned short u16;
typedef unsigned long long u64;
typedef unsigned v2u __attribute__((ext_vector_type(2)));
typedef unsigned v4u __attribute__((ext_vector_type(4)));
typedef int v4i __attribute__((ext_vector_type(4)));
typedef float v2f __attribute__((ext_vector_type(2)));
typedef float v4f __attribute__((ext_vector_type(4)));
typedef short v4s __attribute__((ext_vector_type(4)));
typedef __bf16 v2bf __attribute__((ext_vector_type(2)));
typedef __bf16 v8bf __attribute__((ext_vector_type(8)));
#define GAS __attribute__((address_space(1)))
#define LAS __attribute__((address_space(3)))
#define RLX_AGENT __ATOMIC_RELAXED, __HIP_MEMORY_SCOPE_AGENT
#define DEVINL __device__ __forceinline__

constexpr unsigned WS_FLAG = 4096;
constexpr unsigned FL_U = 0, FL_P = 128, FL_P2 = 256, FL_Y = 384, FL_O = 512;
constexpr unsigned FL_N = 1024;
constexpr unsigned WS_UT = 2u << 20, WS_YT = 5u << 20, WS_PART = 8u << 20, WS_OF = 9u << 20, WS_STAMP = 12u << 20;
constexpr unsigned WS_BYTES = 13u << 20;
constexpr unsigned PART_STRIDE = 3584;
constexpr unsigned OOB = 0x80000000u;
constexpr unsigned L_DUMMY = 162816, L_CNT = 163584;
constexpr unsigned L_KVSLOT = 36992, L_WQTOP = 4 * L_KVSLOT, L_PC = 148480, L_KVC = 152576, L_STB = 25600;
constexpr unsigned L_CS = 4160, L_UOFF = 4 * L_CS, L_CH = 21248;
constexpr unsigned L_KIMG = 0, L_VIMG = 9 * 2576, L_SW = 44032, L_RED = 0, L_QT = 0, L_YT = 20480, L_STA = 40960, L_CONV = 51200, L_CONV5 = L_CONV + 4 * L_CH;
#define CODE_BYTES 43000

struct Params { const u16 *q, *k, *v, *sk, *h, *Dv, *x, *w, *wo; u16* out; char* ws; };

DEVINL v4i mk_rsrc(const void* p, unsigned bytes) {
  u64 a = (u64)p; v4i r; r.x = (int)(unsigned)a; r.y = (int)((unsigned)(a >> 32) & 0xffffu); r.z = (int)bytes; r.w = 0x00020000; return r;
}
DEVINL void dma(v4i rs, unsigned voff, unsigned soff, unsigned lds) {
  asm volatile("s_mov_b32 m0, %3\n\ts_nop 0\n\tbuffer_load_dwordx4 %0, %1, %2 offen lds" :: "v"(voff), "s"(rs), "s"(soff), "s"(lds) : "memory");
}
DEVINL void dma_sc1(v4i rs, unsigned voff, unsigned soff, unsigned lds) {
  asm volatile("s_mov_b32 m0, %3\n\ts_nop 0\n\tbuffer_load_dwordx4 %0, %1, %2 offen sc1 lds" :: "v"(voff), "s"(rs), "s"(soff), "s"(lds) : "memory");
}
DEVINL void warm(v4i rs, unsigned voff, unsigned soff) {
  asm volatile("s_mov_b32 m0, %3\n\ts_nop 0\n\tbuffer_load_dword %0, %1, %2 offen lds" :: "v"(voff), "s"(rs), "s"(soff), "s"(L_DUMMY) : "memory");
}
template <int N> DEVINL void waitv() { asm volatile("s_waitcnt vmcnt(%0)" :: "n"(N) : "memory"); }
DEVINL void rawbar() { asm volatile("s_barrier" ::: "memory"); }
DEVINL void bar() { asm volatile("s_waitcnt lgkmcnt(0)\n\ts_barrier" ::: "memory"); }
#define SCHED() __builtin_amdgcn_sched_barrier(0)
DEVINL v4u lds128(unsigned a) { return *(const LAS v4u*)(size_t)a; }
DEVINL v2u lds64(unsigned a) { return *(const LAS v2u*)(size_t)a; }
DEVINL unsigned lds32(unsigned a) { return *(const LAS unsigned*)(size_t)a; }
DEVINL void sts128(unsigned a, v4u v) { *(LAS v4u*)(size_t)a = v; }
DEVINL void sts64(unsigned a, v2u v) { *(LAS v2u*)(size_t)a = v; }
DEVINL void sts32(unsigned a, unsigned v) { *(LAS unsigned*)(size_t)a = v; }
DEVINL v2u ldtr(unsigned a) { return __builtin_bit_cast(v2u, __builtin_amdgcn_ds_read_tr16_b64_v4i16((LAS v4s*)(size_t)a)); }
DEVINL v4u cat(v2u a, v2u b) { return (v4u){a.x, a.y, b.x, b.y}; }
DEVINL v4f mfma(v4u a, v4u b, v4f c) { return __builtin_amdgcn_mfma_f32_16x16x32_bf16(__builtin_bit_cast(v8bf, a), __builtin_bit_cast(v8bf, b), c, 0, 0, 0); }
DEVINL unsigned pk2(float a, float b) { v2f p = {a, b}; return __builtin_bit_cast(unsigned, __builtin_convertvector(p, v2bf)); }
DEVINL v2u pk4(v4f a) { return (v2u){pk2(a[0], a[1]), pk2(a[2], a[3])}; }
DEVINL float blo(unsigned u) { return __uint_as_float(u << 16); }
DEVINL float bhi(unsigned u) { return __uint_as_float(u & 0xffff0000u); }
DEVINL v2u phi4s(v4f c) {
  float r[4];
#pragma unroll
  for (int j = 0; j < 4; ++j) {
    r[j] = __int_as_float(max(__float_as_int(c[j]), 0x358637bd)) + __builtin_amdgcn_fmed3f(__builtin_amdgcn_exp2f(__builtin_fmaf(c[j], 1.44269504089f, -1.44269504089e-6f)), 0.f, 1.f);
    asm("" : "+v"(r[j]));
  }
  return (v2u){pk2(r[0], r[1]), pk2(r[2], r[3])};
}
DEVINL v2u phi4(v4f c) {
  const v4f a = c * 1.44269504089f - 1.44269504089e-6f;
  v4f r;
#pragma unroll
  for (int j = 0; j < 4; ++j) r[j] = __int_as_float(max(__float_as_int(c[j]), 0x358637bd));
  const v4f e = {__builtin_amdgcn_fmed3f(__builtin_amdgcn_exp2f(a[0]), 0.f, 1.f), __builtin_amdgcn_fmed3f(__builtin_amdgcn_exp2f(a[1]), 0.f, 1.f),
                 __builtin_amdgcn_fmed3f(__builtin_amdgcn_exp2f(a[2]), 0.f, 1.f), __builtin_amdgcn_fmed3f(__builtin_amdgcn_exp2f(a[3]), 0.f, 1.f)};
  return pk4(r + e);
}
DEVINL float add1(float a, float b) { float r; asm("v_add_f32 %0, %1, %2" : "=v"(r) : "v"(a), "v"(b)); return r; }
DEVINL float mul1(float a, float b) { float r; asm("v_mul_f32 %0, %1, %2" : "=v"(r) : "v"(a), "v"(b)); return r; }
DEVINL v4f add4(v4f a, v4f b) { return (v4f){add1(a[0], b[0]), add1(a[1], b[1]), add1(a[2], b[2]), add1(a[3], b[3])}; }
DEVINL v4f mul4(v4f a, v4f b) { return (v4f){mul1(a[0], b[0]), mul1(a[1], b[1]), mul1(a[2], b[2]), mul1(a[3], b[3])}; }
DEVINL float dot2(unsigned a, unsigned b, float c) { return __builtin_amdgcn_fdot2_f32_bf16(__builtin_bit_cast(v2bf, a), __builtin_bit_cast(v2bf, b), c, false); }
DEVINL float dot0(unsigned a, unsigned b) { float r; asm("v_dot2_f32_bf16 %0, %1, %2, 0" : "=v"(r) : "v"(a), "v"(b)); return r; }
#define ST128(v, rs, voff, soff, aux) __builtin_amdgcn_raw_buffer_store_b128(v, rs, (voff) + (soff), 0, aux)
#define PINV(x) asm volatile("" : "+v"(x))
#define PINS(x) asm volatile("" : "+s"(x))

DEVINL void set_flag(char* ws, unsigned idx, u64 tag, int nrep, int lane) {
  const __amdgpu_buffer_rsrc_t rs = __builtin_amdgcn_make_buffer_rsrc(ws, 0, WS_BYTES, 0x00020000);
  const unsigned lo = (unsigned)tag, hi = (unsigned)(tag >> 32);
  if (lane < 8 * nrep) ST128(((v4u){lo, hi, lo, hi}), rs, (lane >> 3) * (FL_N * 128) + (lane & 7) * 16, WS_FLAG + idx * 128, 16);
}
#define NREP 8
#define UDELAY 18
#define YDELAY 8
#define ODELAY 10
#define PDELAY 0
#define AGATE 52
#define OREP 1
#define OSLEEP 2
#define XPOS 4
#define PHINT 1
#define QEARLY 1
#define PHIK phi4s
#define EPRIO 2
#define USTORE 1
#define AUX_A 0
#define AUX_B 0
#define KVDELAY 16
#define OAHEAD 12
#define PSLEEP 2
template <int SL = PSLEEP> DEVINL void wait_flags(char* ws, unsigned idx, int n, u64 tag, int lane) {
  if (lane < n) {
    GAS u64* p = (GAS u64*)(ws + WS_FLAG + (idx + lane) * 128u);
#pragma unroll 1
    for (unsigned spins = 0; spins < (1u << 16); ++spins) {
      if (__hip_atomic_load(p, RLX_AGENT) == tag) break;
      __builtin_amdgcn_s_sleep(SL);
    }
  }
  __builtin_amdgcn_fence(__ATOMIC_ACQUIRE, "wavefront");
  asm volatile("" ::: "memory");
}
DEVINL unsigned cnt_addr(unsigned cnt, int lane) { return lane ? L_DUMMY + 320 + lane * 4 : cnt; }
template <bool DRAIN> DEVINL void arrive_set(unsigned cnt, unsigned n, char* ws, unsigned idx, u64 tag, int lane) {
  unsigned old;
  if (DRAIN) waitv<0>();
  asm volatile("ds_add_rtn_u32 %0, %1, %2\n\ts_waitcnt lgkmcnt(0)" : "=&v"(old) : "v"(cnt_addr(cnt, lane)), "v"(1u) : "memory");
  if (__builtin_amdgcn_readfirstlane(old) == n - 1) set_flag(ws, idx, tag, NREP, lane);
}
#define arrive_flag arrive_set<true>
#define arrive_hint arrive_set<false>
DEVINL void lds_arrive(unsigned cnt, int lane) {
  asm volatile("ds_add_u32 %0, %1" :: "v"(cnt_addr(cnt, lane)), "v"(1u) : "memory");
}
template <int SLEEP> DEVINL void lds_wait(unsigned cnt, unsigned n) {
  unsigned v, sv, spins;
  asm volatile("s_mov_b32 %2, 0x100000\n1:\n\tds_read_b32 %0, %3\n\ts_waitcnt lgkmcnt(0)\n\tv_readfirstlane_b32 %1, %0\n\ts_cmp_ge_u32 %1, %4\n\ts_cbranch_scc1 2f\n\t"
               "s_sleep %5\n\ts_sub_u32 %2, %2, 1\n\ts_cbranch_scc0 1b\n2:"
               : "=&v"(v), "=&s"(sv), "=&s"(spins) : "v"(cnt), "s"(n), "n"(SLEEP) : "memory", "scc");
}
#define ST(i)
#define STC(i)
#define STARG
#define STPASS

DEVINL void gemm_kv(v4f (&aK)[8], v4f (&aV)[8], v4f (&aT)[4], unsigned xa, unsigned xb, unsigned wa, unsigned wt STARG) {
  v4u xp[8], wp[3], xq[8], wq[3];
  unsigned so = L_KVSLOT;
#define RD(X, W, hs) do { X[0] = lds128((xa + so) ^ ((hs) * 64)); \
    W[0] = cat(ldtr(wa + so + (hs) * 5120), ldtr(wa + so + (hs) * 5120 + 2560)); \
    W[1] = cat(ldtr(wa + so + (hs) * 5120 + 10256), ldtr(wa + so + (hs) * 5120 + 2560 + 10256)); \
    W[2] = cat(ldtr(wt + so + (hs) * 5120), ldtr(wt + so + (hs) * 5120 + 2560)); \
    _Pragma("unroll") for (int m = 1; m < 8; ++m) X[m] = lds128((((m < 4 ? xa : xb) + so) ^ ((hs) * 64)) + (m & 3) * 2048); } while (0)
#define MM(X, W) do { _Pragma("unroll") for (int m = 0; m < 8; ++m) { aK[m] = mfma(X[m], W[0], aK[m]); aV[m] = mfma(X[m], W[1], aV[m]); if (m < 4) aT[m] = mfma(X[m], W[2], aT[m]); } } while (0)
#define MIX() do { _Pragma("unroll") for (int i = 0; i < 20; ++i) { __builtin_amdgcn_sched_group_barrier(0x8, 1, 0); __builtin_amdgcn_sched_group_barrier(0x100, 1, 0); } } while (0)
  bar(); SCHED();
  RD(xp, wp, 0);
  SCHED();
#pragma unroll 1
  for (int s = 0; s < 9; ++s) {
    RD(xq, wq, 1); MM(xp, wp); MIX(); SCHED();
    bar(); SCHED();
    so = (so == 0) ? 3 * L_KVSLOT : so - L_KVSLOT;
    RD(xp, wp, 0); MM(xq, wq); MIX(); SCHED();
  }
  RD(xq, wq, 1); MM(xp, wp); MIX(); SCHED();
  ST(1);
#pragma unroll
  for (int m = 0; m < 4; ++m) aT[m] = mfma(xq[m], wq[2], aT[m]);
#pragma unroll
  for (int m = 0; m < 8; ++m) aK[m] = mfma(xq[m], wq[0], aK[m]);
#pragma unroll
  for (int m = 0; m < 8; ++m) aV[m] = mfma(xq[m], wq[1], aV[m]);
#undef RD
#undef MM
#undef MIX
}
struct ConvArgs { unsigned ha, ua, yo; v4u dg; };
DEVINL ConvArgs conv_args(unsigned cb, unsigned dbits, unsigned yoff, int lane) {
  const int i = lane & 15, g = lane >> 4, jt = 15 - i - 8 * g;
  ConvArgs c;
  c.ha = cb + (i >> 2) * L_CS + 16 * g + 8 * (i & 3) + 8;
  c.ua = cb + L_UOFF + 480 - 32 * i + 16 * g;
  c.yo = yoff + 16 * i + 256 * (g >> 1);
#pragma unroll
  for (int d = 0; d < 4; ++d) c.dg[d] = (jt == 2 * d) ? dbits : (jt == 2 * d + 1) ? (dbits << 16) : 0u;
  PINV(c.ha); PINV(c.ua); PINV(c.yo); PINV(c.dg);
  return c;
}
DEVINL v4u conv_pack(v4f a, v4f b) {
  v2u X = pk4(a), Y = pk4(b);
  auto r0 = __builtin_amdgcn_permlane16_swap(X.x, Y.x, false, false);
  auto r1 = __builtin_amdgcn_permlane16_swap(X.y, Y.y, false, false);
  return (v4u){r0[0], r1[0], r0[1], r1[1]};
}
DEVINL void conv_store(v4u o, int ta, unsigned yo, char* ws, int lane) {
  const __amdgpu_buffer_rsrc_t rs = __builtin_amdgcn_make_buffer_rsrc(ws, 0, WS_BYTES, 0x00020000);
  ST128(o, rs, yo + ((lane & 16) ? 7 - ta : ta) * 512, 0, 16);
}
DEVINL void conv_wave(int off5, ConvArgs c, char* ws, int wv, int lane STARG) {
  unsigned ha = c.ha + off5, ua = c.ua + off5;
  v4f acc[8], ac5[8];
  v4u F[8], G[8], F2[8], G2[8];
#define CRD(F, G) do { F[0] = cat(ldtr(ha), ldtr(ha + 8)); _Pragma("unroll") for (int v = 0; v < 8; ++v) G[v] = lds128(ua + (7 - v) * 512); \
    _Pragma("unroll") for (int v = 1; v < 8; ++v) F[v] = cat(ldtr(ha + v * 512), ldtr(ha + v * 512 + 8)); ha += 64; ua += 64; PINV(ha); PINV(ua); } while (0)
#define CMM(A, F, G) do { _Pragma("unroll") for (int w = 0; w < 8; ++w) _Pragma("unroll") for (int t = w; t < 8; ++t) A[t] = mfma(F[w], G[t - w], A[t]); } while (0)
#define CMIX() do { _Pragma("unroll") for (int j = 0; j < 36; ++j) { __builtin_amdgcn_sched_group_barrier(0x8, 1, 0); __builtin_amdgcn_sched_group_barrier(0x100, 1, 0); } } while (0)
  CRD(F, G);
  SCHED();
#pragma unroll
  for (int t = 0; t < 8; ++t) ac5[t] = (v4f){0, 0, 0, 0};
  CRD(F2, G2); CMM(ac5, F, G); CMIX(); SCHED();
  waitv<0>(); lds_wait<0>(L_CNT + 60, 4);
  ha = c.ha; ua = c.ua;
  CRD(F, G); CMM(ac5, F2, G2); CMIX(); SCHED();
  {
    unsigned ra = L_RED + wv * 1024 + lane * 16;
#pragma unroll
    for (int t = 0; t < 8; ++t) acc[t] = mfma(c.dg, G[t], (v4f){0, 0, 0, 0});
    CRD(F2, G2); CMM(acc, F, G);
#pragma unroll
    for (int t = 0; t < 8; ++t) sts128(ra + t * 4096, __builtin_bit_cast(v4u, ac5[t]));
#pragma unroll
    for (int j = 0; j < 24; ++j) { __builtin_amdgcn_sched_group_barrier(0x8, 1, 0); __builtin_amdgcn_sched_group_barrier(0x100, 1, 0); }
#pragma unroll
    for (int j = 0; j < 8; ++j) { __builtin_amdgcn_sched_group_barrier(0x8, 2, 0); __builtin_amdgcn_sched_group_barrier(0x200, 1, 0); }
    SCHED();
    lds_arrive(L_CNT + 12, lane);
  }
  ST(16);
#pragma unroll 1
  for (int it = 0; it < 3; ++it) {
    CRD(F, G); CMM(acc, F2, G2); CMIX(); SCHED();
    CRD(F2, G2); CMM(acc, F, G); CMIX(); SCHED();
  }
  ST(17);
  bar(); SCHED();
#undef CRD
#undef CMM
#undef CMIX
#pragma unroll
  for (int a = 0; a < 4; ++a) {
    const int b = 7 - a;
#pragma unroll
    for (int w = 0; w <= b; ++w) { acc[b] = mfma(F2[w], G2[b - w], acc[b]); if (w <= a) acc[a] = mfma(F2[w], G2[a - w], acc[a]); }
  }
#pragma unroll
  for (int a = 0; a < 4; ++a) conv_store(conv_pack(acc[a], acc[7 - a]), a, c.yo, ws, lane);
  __builtin_amdgcn_sched_group_barrier(0x8, 12, 0);
#pragma unroll
  for (int a = 0; a < 3; ++a) {
#pragma unroll
    for (int j = 0; j < 8; ++j) { __builtin_amdgcn_sched_group_barrier(0x2, 1, 0); __builtin_amdgcn_sched_group_barrier(0x8, 1, 0); }
    __builtin_amdgcn_sched_group_barrier(0x40, 1, 0);
  }
}

__global__ void __launch_bounds__(512, 2) fused_kernel(Params p) {
  __shared__ __attribute__((aligned(1024))) char smem[163840];
  u64 pc; asm volatile("s_getpc_b64 %0" : "=s"(pc));
  const int tid = threadIdx.x, lane = tid & 63;
  const int b = blockIdx.x, xcd = b & 7, bi = b >> 3;
  warm(mk_rsrc((const void*)pc, CODE_BYTES), tid < 42 ? ((bi & 7) * 42 + tid) * 128 : OOB, 0);
  const int wv = __builtin_amdgcn_readfirstlane(tid >> 6);
  const bool isA = bi >= 16;
  const int rt = 4 * (xcd >> 1) + (bi & 3), g = 4 * (xcd & 1) + ((bi >> 2) & 3);
  ST(0);
  typedef __attribute__((address_space(4))) const u64 cu64;
  typedef __attribute__((address_space(4))) const unsigned cu32;
  const int lw = wv & 3, iq = bi & 15;
  int k0 = ((xcd >> 1) * 5 >> 1) + 5 * (xcd & 1); k0 = k0 >= 10 ? k0 - 10 : k0;
  v4i rx, rw;
#define MK_XW() do { const u16 *x_ = p.x, *w_ = p.w; PINS(x_); PINS(w_); rx = mk_rsrc(x_, 2048 * 1280); rw = mk_rsrc(w_, 640 * 3840); } while (0)
  auto off_x = [&](int j) {
    int ka = k0 + 2 * j + (lane >> 5); ka = ka >= 10 ? ka - 10 : ka;
    unsigned vo = ((xcd >> 1) * 512 + iq * 32 + (lane & 31)) * 1280 + ka * 128; PINV(vo);
    return vo;
  };
  auto off_w = [&](int j) {
    const unsigned r10 = lane / 10, c10 = lane - r10 * 10, rho = 6 * j + r10; int kb = k0 + (rho >> 2); kb = kb >= 10 ? kb - 10 : kb;
    unsigned vo = (kb * 64 + iq * 4 + (rho & 3)) * 3840 + 1280 + (xcd & 1) * 640 + c10 * 128 + (c10 >= 5 ? 640 : 0); PINV(vo);
    vo = (lane < 60 && rho < 40) ? vo : OOB; PINV(vo);
    return vo;
  };
  auto warm_x = [&](int j) { warm(rx, off_x(j), 0); };
  auto warm_w = [&](int j) { warm(rw, off_w(j), 0); };
  unsigned xo0 = 0, xo1 = 0, wo_[5] = {0, 0, 0, 0, 0};
  auto loader_offsets = [&]() {
    const unsigned r = lane >> 3;
    xo0 = (rt * 128 + lw * 32 + r) * 1280 + (((lane & 7) ^ (r >> 1)) << 4);
    xo1 = (xo0 ^ 64) + 8 * 1280;
#pragma unroll
    for (int j = 0; j < 5; ++j) { const unsigned ch = j * 64 + lane, rho = ch / 10, c = ch - rho * 10;
      wo_[j] = (8 * ((rho >> 2) & 3) + 4 * (rho >> 4) + (rho & 3)) * 3840 + c * 16; PINV(wo_[j]); }
    PINV(xo0); PINV(xo1);
  };
  const unsigned kwcol = (640 + 640 * (lw & 1) + 80 * g) * 2 + (lw >> 1) * 32 * 3840, kwl = 16384 + (lw & 1) * 10256 + (lw >> 1) * 5120;
  auto issue_x = [&](int ks, unsigned so) {
#pragma unroll
    for (int j = 0; j < 4; ++j) dma(rx, (j & 1) ? xo1 : xo0, ks * 128 + (j >> 1) * 16 * 1280, so + lw * 4096 + j * 1024);
  };
  auto issue_kw = [&](int ks, unsigned so) {
#pragma unroll
    for (int j = 0; j < 5; ++j) dma(rw, wo_[j], kwcol + ks * 64 * 3840, so + kwl + j * 1024);
  };
  const int tt = 4 * xcd + (iq >> 2), cq = iq & 3, t0 = tt * 64;
  if (isA) {
    const unsigned r20 = lane / 20, so = (t0 + wv * 8) * 1280, la = L_KIMG + 2576 + wv * 2576;
    unsigned vo = r20 * 1280 + (lane - r20 * 20) * 16 + cq * 320, xv = off_x(1); PINV(vo);
    MK_XW();
    if (wv == 0) warm(rx, xv, 0);
    if (KVDELAY) __builtin_amdgcn_s_sleep(KVDELAY);
    const v4i rk = mk_rsrc(p.k, 2048 * 1280), rv = mk_rsrc(p.v, 2048 * 1280);
    if (lane < 60) {
      dma(rk, vo, so, la); dma(rk, vo, so + 3840, la + 960); dma(rv, vo, so, la + 8 * 2576); dma(rv, vo, so + 3840, la + 8 * 2576 + 960);
      if (wv == 7) dma(rk, tt ? vo + (t0 - 3) * 1280 : OOB, 0, L_KIMG + 1600);
      if (lane < 40) {
        dma(rk, vo, so + 7680, la + 1920); dma(rv, vo, so + 7680, la + 8 * 2576 + 1920);
        if (wv == 5 || wv == 6) dma(mk_rsrc(p.sk, 640 * 8), cq * 1280 + (wv - 5) * 640 + lane * 16, 0, L_SW + (wv - 5) * 640);
      }
    }
  } else if (wv >= 4) { loader_offsets(); MK_XW(); issue_x(k0, L_KVSLOT); issue_kw(k0, L_KVSLOT); }
  else { const unsigned xv = off_x(0), wz = off_w(wv - 1); MK_XW(); warm(wv == 0 ? rx : rw, wv == 0 ? xv : wv < 3 ? wz : OOB, 0);
    if (PHINT) ST128(((v4u){~0u, ~0u, ~0u, ~0u}), __builtin_amdgcn_make_buffer_rsrc(p.ws, 0, WS_BYTES, 0x00020000),
                                                      lane < 56 ? lane * 16 : OOB, WS_PART + (g * 16 + rt) * PART_STRIDE + wv * 896, 16);
  }
  ST(12);
  asm volatile("" ::: "memory");
  const int c5 = 80 * g + 5 * rt, c0 = c5 + lw, cd = wv < 4 ? c0 : c5 + 4;
  const v4i rws = mk_rsrc(p.ws, WS_BYTES), ro = mk_rsrc(p.wo, 640 * 1280);
  const __amdgpu_buffer_rsrc_t bws = __builtin_amdgcn_make_buffer_rsrc(p.ws, 0, WS_BYTES, 0x00020000);
  const __amdgpu_buffer_rsrc_t bout = __builtin_amdgcn_make_buffer_rsrc(p.out, 0, 2048 * 2560, 0x00020000);
  const unsigned rep = (xcd & (NREP - 1)) * FL_N;
  const int l15 = lane & 15, grp = lane >> 4;
  const unsigned trr = (4 * grp + (l15 >> 2)) * 160, trl = trr + 8 * (lane & 3);
  const int gr0 = (4 * xcd + ((bi >> 2) & 3)) * 64, gct = (bi & 3) + 4 * (bi >> 4), rot = bi & 3;
  unsigned vo5[5];
  auto row_offsets = [&]() {
#pragma unroll
    for (int j = 0; j < 5; ++j) { const unsigned ch = j * 64 + lane, r = ch / 10, c = ch - r * 10; vo5[j] = r * 1280 + c * 16; PINV(vo5[j]); }
  };
  unsigned oo3[3];
  auto out_offsets = [&]() {
#pragma unroll
    for (int j = 0; j < 3; ++j) { const unsigned ch = j * 64 + lane, r = ch / 10; oo3[j] = ch < 160 ? (lw * 16 + r) * 2560 + (ch - r * 10) * 16 : OOB; PINV(oo3[j]); }
  };
#define OUT_TILE(res, sta, soff, aux) do { const unsigned w_ = (sta) + l15 * 160 + grp * 8; \
    _Pragma("unroll") for (int n = 0; n < 5; ++n) sts64(w_ + n * 32, res[n]); \
    asm volatile("" ::: "memory"); \
    _Pragma("unroll") for (int j = 0; j < 3; ++j) ST128(lds128((sta) + j * 1024 + lane * 16), bout, oo3[j], soff, aux); } while (0)
  u64 tag; unsigned dbits;
#define GET_TAG() do { const char* w_ = p.ws; const u16* d_ = p.Dv; PINS(w_); PINS(d_); u64 e_ = *(cu64*)w_; dbits = ((cu32*)d_)[cd >> 1]; PINS(e_); PINS(dbits); \
    dbits = (dbits >> (16 * (cd & 1))) & 0xffffu; tag = e_ * 6364136223846793005ull + 1442695040888963407ull; PINS(tag); } while (0)
  if (tid == 0) { asm volatile("" :: "v"(smem)); sts128(L_CNT, (v4u){0, 0, 0, 0}); sts128(L_CNT + 16, (v4u){0, 0, 0, 0}); sts128(L_CNT + 32, (v4u){0, 0, 0, 0}); sts128(L_CNT + 48, (v4u){0, 0, 0, 0}); }

  if (!isA) {
    unsigned vw0, vw4;
    auto wo_offsets = [&]() { unsigned l_ = lane; PINV(l_); const unsigned c0_ = lw * 64 + l_, r0 = c0_ / 10, c4 = 256 + l_, r4 = c4 / 10;
      vw0 = r0 * 1280 + (c0_ - r0 * 10) * 16; vw4 = r4 * 1280 + (c4 - r4 * 10) * 16; PINV(vw0); PINV(vw4); };
    auto load_wo = [&](int y, unsigned base) {
      int gb = gct; PINS(gb);
      dma(ro, vw4, ((y ^ rot) * 5 + lw) * 32 * 1280 + gb * 160, base + lw * 5120 + 4096);
      if (y == 0 || lw == y) dma(ro, vw4, ((y ^ rot) * 5 + 4) * 32 * 1280 + gb * 160, base + 4 * 5120 + 4096);
#pragma unroll
      for (int i = 0; i < 5; ++i) dma(ro, vw0, ((y ^ rot) * 5 + i) * 32 * 1280 + gb * 160, base + i * 5120 + lw * 1024);
    };
    if (wv < 4) {
      GET_TAG();
      const int hf = wv & 1;
      const bool isk = !(l15 & 4), own = (l15 >> 2) == 2 * hf + 1;
      const unsigned pc0 = L_PC + wv * 896, trash = L_DUMMY + 256;
      const int jb = own ? (l15 & 3) : l15 == 0 ? 4 : -1;
      unsigned pb = jb >= 0 ? pc0 + (80 + 5 * grp + jb) * 8 : trash, pcc = grp == 2 * hf ? pc0 + (64 + l15) * 8 : trash,
               pd = (grp == 2 * hf && jb >= 0) ? pc0 + (100 + jb) * 8 : trash;
      PINV(pb); PINV(pcc); PINV(pd);
      out_offsets(); wo_offsets();
      unsigned po = (tid < 224) ? WS_PART + g * 16 * PART_STRIDE + tid * 16 : OOB, pso = lane < 56 ? WS_PART + (g * 16 + rt) * PART_STRIDE + wv * 896 + lane * 16 : OOB;
      PINV(po); PINV(pso);
      if (lane < 7) sts64(pc0 + (105 + lane) * 8, (v2u){0, 0});
      v4f aK[8], aV[8], aT[4];
#pragma unroll
      for (int m = 0; m < 8; ++m) { aK[m] = (v4f){1e-6f, 1e-6f, 1e-6f, 1e-6f}; aV[m] = (v4f){0, 0, 0, 0}; }
#pragma unroll
      for (int m = 0; m < 4; ++m) { const float c = isk ? 1e-6f : 0.f; aT[m] = (v4f){c, c, c, c}; }
      const unsigned x0 = l15 * 128 + ((grp ^ (l15 >> 1)) << 4);
      gemm_kv(aK, aV, aT, x0 + hf * 8192, x0 + (1 - hf) * 8192, 16384 + trl + wv * 40, 16384 + trr + (lane & 1) * 10256 + (wv >> 1) * 80 + (lane & 2) * 20 + 32 STPASS);
      if (EPRIO) __builtin_amdgcn_s_setprio(EPRIO);
      v4u tf[4], kf[4], vf[4];
#pragma unroll
      for (int t = 0; t < 2; ++t) {
        const v4u a = cat(PHIK(aT[2 * t]), PHIK(aT[2 * t + 1])), c = cat(pk4(aT[2 * t]), pk4(aT[2 * t + 1]));
#pragma unroll
        for (int d = 0; d < 4; ++d) tf[t][d] = isk ? a[d] : c[d];
        sts128(L_KVSLOT + wv * 2048 + t * 1024 + lane * 16, tf[t]);
      }
      asm volatile("" ::: "memory");
      sts32(L_CNT + 32 + wv * 4, 1u); lds_arrive(L_CNT + 52, lane);
      v4f dA = {0, 0, 0, 0}, dB = dA, dC = dA, dD = dA;
      auto kvstep = [&](int t) {
        v4u tb;
#pragma unroll
        for (int d = 0; d < 4; ++d) tb[d] = own ? tf[t][d] : 0x3f803f80u;
        dA = mfma(kf[t], vf[t], dA); dB = mfma(kf[t], tb, dB); dC = mfma(tf[t], vf[t], dC); dD = mfma(tf[t], tb, dD);
      };
#pragma unroll
      for (int t = 0; t < 3; ++t) { kf[t] = cat(PHIK(aK[2 * t]), PHIK(aK[2 * t + 1])); vf[t] = cat(pk4(aV[2 * t]), pk4(aV[2 * t + 1])); }
      kvstep(0);
      PINV(kf[2]); PINV(vf[2]);
      asm volatile("" ::: "memory");
      unsigned mark = lds32(L_CNT + 32 + (wv ^ 1) * 4);
      asm volatile("" ::: "memory");
      tf[2] = lds128(L_KVSLOT + (wv ^ 1) * 2048 + lane * 16); tf[3] = lds128(L_KVSLOT + (wv ^ 1) * 2048 + 1024 + lane * 16);
      asm volatile("" ::: "memory");
      kf[3] = cat(PHIK(aK[6]), PHIK(aK[7])); vf[3] = cat(pk4(aV[6]), pk4(aV[7]));
      kvstep(1);
      PINV(kf[3]); PINV(vf[3]);
      if (__builtin_amdgcn_readfirstlane(mark) == 0) {
        lds_wait<0>(L_CNT + 32 + (wv ^ 1) * 4, 1);
        tf[2] = lds128(L_KVSLOT + (wv ^ 1) * 2048 + lane * 16); tf[3] = lds128(L_KVSLOT + (wv ^ 1) * 2048 + 1024 + lane * 16);
      }
      kvstep(2); kvstep(3);
      sts64(pc0 + lane * 8, pk4(dA)); sts64(pb, pk4(dB)); sts64(pcc, pk4(dC)); sts64(pd, pk4(dD));
      waitv<0>();
      ST128(lds128(pc0 + lane * 16), bws, pso, 0, 16);
      if (PHINT) arrive_hint(L_CNT, 4, p.ws, FL_P + g * 16 + rt, tag, lane); else lds_arrive(L_CNT, lane);
      __builtin_amdgcn_s_setprio(0);
      ST(2);
      lds_wait<1>(L_CNT + 28, 4);
      load_wo(1, L_WQTOP - 25600);
      lds_wait<1>(L_CNT + 60, 4);
      load_wo(2, L_WQTOP - 51200);
      if (PDELAY) __builtin_amdgcn_s_sleep(PDELAY);
      ST(4);
      wait_flags(p.ws, rep + FL_P + g * 16, 16, tag, lane);
      waitv<0>();
      ST(6);
#pragma unroll 1
      for (int pass = 0; pass < 2; ++pass) {
        v4u r[16];
#pragma unroll
        for (int j = 0; j < 16; ++j) { r[j] = __builtin_amdgcn_raw_buffer_load_b128(bws, po, ((j + 4 * bi) & 15) * PART_STRIDE, 16); SCHED(); }
        if (pass == 0) arrive_hint(L_CNT + 4, 4, p.ws, FL_P2 + g * 16 + rt, tag, lane);
        v4f s0 = {0, 0, 0, 0}, s1 = s0;
        unsigned sel0 = 0x00003f80u, sel1 = 0x3f800000u; PINV(sel0); PINV(sel1);
        auto dot = [](unsigned x, unsigned sel, float c) { return __builtin_amdgcn_fdot2_f32_bf16(__builtin_bit_cast(v2bf, x), __builtin_bit_cast(v2bf, sel), c, false); };
#pragma unroll
        for (int j = 0; j < 16; ++j) { s0[0] = dot(r[j].x, sel0, s0[0]); s0[1] = dot(r[j].x, sel1, s0[1]); s0[2] = dot(r[j].y, sel0, s0[2]); s0[3] = dot(r[j].y, sel1, s0[3]);
          s1[0] = dot(r[j].z, sel0, s1[0]); s1[1] = dot(r[j].z, sel1, s1[1]); s1[2] = dot(r[j].w, sel0, s1[2]); s1[3] = dot(r[j].w, sel1, s1[3]); }
        sts128(L_KVC + tid * 16, cat(pk4(s0), pk4(s1)));
        if (!PHINT) break;
        const float chk = (s0[0] + s0[2]) + (s1[0] + s1[2]);
        if (!__any(chk != chk)) break;
        wait_flags(p.ws, rep + FL_P2 + g * 16, 16, tag, lane);
      }
      lds_arrive(L_CNT + 8, lane);
      ST(17);
      lds_wait<0>(L_CNT + 48, 4);
      load_wo(3, L_WQTOP - 76800);
      waitv<0>(); lds_arrive(L_CNT + 24, lane);
      lds_wait<2>(L_CNT + 24, 8);
    } else {
      auto piece = [&](int q) {
        const int st = q / 9, j = q % 9; const int ks = k0 + st >= 10 ? k0 + st - 10 : k0 + st; const unsigned so = ((st & 3) ^ 1) * L_KVSLOT;
        if (j < 4) dma(rx, (j & 1) ? xo1 : xo0, ks * 128 + (j >> 1) * 16 * 1280, so + lw * 4096 + j * 1024);
        else dma(rw, wo_[j - 4], kwcol + ks * 64 * 3840, so + kwl + (j - 4) * 1024);
      };
      auto wq_block = [&](int j, int i0, int i1) {
        int kb = 2 * k0 + j; kb = kb >= 20 ? kb - 20 : kb;
#pragma unroll
        for (int i = i0; i < i1; ++i) dma(rw, wo_[i], kb * 32 * 3840 + 160 * g, L_WQTOP - (j + 1) * 5120 + i * 1024);
      };
      GET_TAG();
      v4u xr[20][2];
      unsigned xa = (lw * 32 + l15) * 128 + ((grp ^ (l15 >> 1)) << 4), xb = xa + 2 * L_KVSLOT, xc = xa ^ 64, xd = xb ^ 64; PINV(xa); PINV(xb); PINV(xc); PINV(xd);
#define XRD(s) do { _Pragma("unroll") for (int hs = 0; hs < 2; ++hs) _Pragma("unroll") for (int m = 0; m < 2; ++m) \
        xr[2 * (s) + hs][m] = lds128((((s) & 2) ? (hs ? xd : xb) : (hs ? xc : xa)) + (((s) & 1) ^ 1) * L_KVSLOT + m * 2048); SCHED(); } while (0)
#define KVSTEP(s, q0, q1) do { _Pragma("unroll") for (int q = (q0); q < (q1); ++q) { piece(q); if ((s) > 0 && q == (q0) + XPOS) XRD((s) > 0 ? (s) - 1 : 0); } waitv<(q1) - 9 * ((s) + 1)>(); bar(); } while (0)
      KVSTEP(0, 9, 13); ST(14);
      KVSTEP(1, 13, 23); KVSTEP(2, 23, 33); KVSTEP(3, 33, 43); KVSTEP(4, 43, 53);
      KVSTEP(5, 53, 63); KVSTEP(6, 63, 73); KVSTEP(7, 73, 83); KVSTEP(8, 83, 90);
#undef KVSTEP
      ST(15);
      wq_block(lw, 0, 5); XRD(8); wq_block(4 + lw, 0, 2);
      waitv<7>(); bar();
      wq_block(4 + lw, 2, 5);
      XRD(9);
#undef XRD
      ST(13);
      wq_block(8 + lw, 0, 5); wq_block(12 + lw, 0, 5); wq_block(16 + lw, 0, 5);
      waitv<10>();
      lds_arrive(L_CNT + 12, lane);
      v4f acc[2][5];
#pragma unroll
      for (int m = 0; m < 2; ++m)
#pragma unroll
        for (int n = 0; n < 5; ++n) acc[m][n] = (v4f){1e-6f, 1e-6f, 1e-6f, 1e-6f};
      unsigned wa0 = L_WQTOP - 51200 + trl, wt0 = L_WQTOP - 51200 + trr + (lane & 3) * 40 + 32, wa1 = wa0 - 51200, wt1 = wt0 - 51200;
      PINV(wa0); PINV(wt0); PINV(wa1); PINV(wt1);
      lds_wait<0>(L_CNT + 12, 4);
      ST(1);
      v4u wf[2][5];
#define QRD(j) do { const unsigned o_ = (9 - (j) % 10) * 5120; _Pragma("unroll") for (int n = 0; n < 5; ++n) { \
        const unsigned a_ = n < 4 ? ((j) < 10 ? wa0 : wa1) + n * 40 + o_ : ((j) < 10 ? wt0 : wt1) + o_; wf[(j) & 1][n] = cat(ldtr(a_), ldtr(a_ + 2560)); } } while (0)
      QRD(0);
      if (!QEARLY) lds_wait<1>(L_CNT, 4);
      SCHED();
      ST(2);
#pragma unroll
      for (int j = 0; j < 20; ++j) {
        if (j < 19) QRD(j + 1);
#pragma unroll
        for (int n = 0; n < 5; ++n)
#pragma unroll
          for (int m = 0; m < 2; ++m) acc[m][n] = mfma(wf[j & 1][n], xr[j][m], acc[m][n]);
#pragma unroll
        for (int i = 0; i < 10; ++i) { __builtin_amdgcn_sched_group_barrier(0x8, 1, 0); __builtin_amdgcn_sched_group_barrier(0x100, 1, 0); }
        SCHED();
        if (j == 4) { waitv<0>(); lds_arrive(L_CNT + 52, lane); }
        if (j == 10) { wo_offsets(); lds_wait<0>(L_CNT + 52, 8); load_wo(0, 0); SCHED(); }
        if (j == 3 || j == 8 || j == 13) lds_arrive(j == 3 ? L_CNT + 28 : j == 8 ? L_CNT + 60 : L_CNT + 48, lane);
      }
#undef QRD
      ST(3);
      v2u pq[2][5];
#pragma unroll
      for (int m = 0; m < 2; ++m)
#pragma unroll
        for (int n = 0; n < 5; ++n) pq[m][n] = phi4(acc[m][n]);
      v4u sq[2][4];
#pragma unroll
      for (int m = 0; m < 2; ++m)
#pragma unroll
        for (int h = 0; h < 4; ++h) { sq[m][h] = cat(pq[m][h], pq[m][4]); PINV(sq[m][h]); }
      const int gam = l15 >> 2, rr = l15 & 3;
      unsigned aA = L_KVC + lane * 8, aC = L_KVC + grp * 896 + (64 + l15) * 8, aB = L_KVC + gam * 896 + (80 + 5 * grp + rr) * 8, aZ = L_KVC + rr * 896 + (84 + 5 * grp) * 8,
               aD = L_KVC + (grp == gam ? gam * 896 + (100 + rr) * 8 : 3584), aE = L_KVC + (grp == rr ? rr * 896 + 104 * 8 : 3584);
      PINV(aA); PINV(aC); PINV(aB); PINV(aZ); PINV(aD); PINV(aE);
      waitv<0>();
      ST(4);
      __builtin_amdgcn_s_setprio(3);
      lds_wait<0>(L_CNT + 8, 4);
      ST(7);
      {
        const v2u fz = lds64(aZ), fe = lds64(aE), fc = lds64(aC), fb = lds64(aB), fd = lds64(aD), z2 = {0, 0};
        v4u fo[4], ft[4], fn[4];
#pragma unroll
        for (int h = 0; h < 4; ++h) {
          fn[h] = cat(rr == h ? fz : z2, h == 0 ? fe : z2);
          fo[h] = cat(lds64(aA + h * 896), grp == h ? fc : z2);
          ft[h] = cat(gam == h ? fb : z2, h == 0 ? fd : z2);
        }
#pragma unroll
        for (int m = 0; m < 2; ++m) {
          v4f den = {1e-6f, 1e-6f, 1e-6f, 1e-6f}, nt = {0, 0, 0, 0}, no[4];
#pragma unroll
          for (int h = 0; h < 4; ++h) den = mfma(fn[h], sq[m][h], den);
#pragma unroll
          for (int h = 0; h < 4; ++h) { no[h] = mfma(fo[h], sq[m][h], (v4f){0, 0, 0, 0}); nt = mfma(ft[h], sq[m][h], nt); }
          const float r0 = __builtin_amdgcn_rcpf(den[0]), r1 = __builtin_amdgcn_rcpf(den[1]), r2 = __builtin_amdgcn_rcpf(den[2]), r3 = __builtin_amdgcn_rcpf(den[3]);
          const float rg = grp == 0 ? r0 : grp == 1 ? r1 : grp == 2 ? r2 : r3;
          const unsigned oo = WS_OF + ((rt * 8 + 2 * lw + m) * 8 + g) * 2560;
          ST128(cat(pk4(no[0] * r0), pk4(no[1] * r1)), bws, lane * 16, oo, 16);
          ST128(cat(pk4(no[2] * r2), pk4(no[3] * r3)), bws, lane * 16 + 1024, oo, 16);
          __builtin_amdgcn_raw_buffer_store_b64(pk4(nt * rg), bws, lane * 8 + 2048, oo, 16);
        }
        lds_arrive(L_CNT + 24, lane);
        ST(8);
        waitv<0>();
        set_flag(p.ws, FL_O + (rt * 4 + lw) * 8 + g, tag, 1, lane);
        lds_arrive(L_CNT + 16, lane);
      }
      ST(11);
    }
  } else {
    if (wv < 4) {
      GET_TAG();
      out_offsets();
      const ConvArgs ca = conv_args(L_CONV + wv * L_CH, dbits, WS_YT + c0 * 4096, lane);
      waitv<0>(); bar();
      ST(1);
      {
        const int tg = lane & 7;
        unsigned ka = L_KIMG + tg * 2576 + (8 * wv + (lane >> 3)) * 4, sa = L_SW + (8 * wv + (lane >> 3)) * 16;
        unsigned uo = WS_UT + (160 * cq + 16 * wv + 2 * (lane >> 3)) * 4096 + 2 * (2040 - t0 - 8 * tg);
        unsigned kr[11], vr[8]; v4u sw;
#define PRD() do { PINV(ka); sw = lds128(sa); _Pragma("unroll") for (int j = 0; j < 11; ++j) kr[j] = lds32(ka + (j < 3 ? (5 + j) * 320 : 2576 + (j - 3) * 320)); \
          _Pragma("unroll") for (int j = 0; j < 8; ++j) vr[j] = lds32(ka + (L_VIMG - L_KIMG) + j * 320); ka += 128; sa += 512; } while (0)
        PRD();
        v4u ua[3], ub[3];
        ua[2] = ub[2] = (v4u){0, 0, 0, 0};
#pragma unroll
        for (int it = 0; it < 3; ++it) {
          if (it == 2 && wv >= 2) break;
          unsigned pa[10], pb[10]; v2f vv[8];
#pragma unroll
          for (int j = 0; j < 10; ++j) { pa[j] = __builtin_amdgcn_perm(kr[j], kr[j + 1], 0x05040100u); pb[j] = __builtin_amdgcn_perm(kr[j], kr[j + 1], 0x07060302u); }
#pragma unroll
          for (int j = 0; j < 8; ++j) vv[j] = (v2f){blo(vr[j]), bhi(vr[j])};
          const v4u w = sw;
          SCHED();
          if (it < 2) PRD();
          SCHED();
          v2f uu[8];
#pragma unroll
          for (int j = 0; j < 8; ++j) uu[j] = (v2f){dot0(pa[j], w.y), dot0(pb[j], w.w)};
          SCHED();
#pragma unroll
          for (int j = 0; j < 8; ++j) uu[j] = (v2f){dot2(pa[j + 2], w.x, uu[j].x), dot2(pb[j + 2], w.z, uu[j].y)};
          SCHED();
#pragma unroll
          for (int j = 0; j < 8; ++j) uu[j] *= vv[j];
          SCHED();
          ua[it] = (v4u){pk2(uu[7].x, uu[6].x), pk2(uu[5].x, uu[4].x), pk2(uu[3].x, uu[2].x), pk2(uu[1].x, uu[0].x)};
          ub[it] = (v4u){pk2(uu[7].y, uu[6].y), pk2(uu[5].y, uu[4].y), pk2(uu[3].y, uu[2].y), pk2(uu[1].y, uu[0].y)};
          PINV(ua[it]); PINV(ub[it]);
          SCHED();
          if (USTORE) { ST128(ua[it], bws, uo, it * 64 * 4096, 16); ST128(ub[it], bws, uo, (it * 64 + 1) * 4096, 16); SCHED(); }
        }
        ST(13);
        if (!USTORE) {
#pragma unroll
          for (int it = 0; it < 3; ++it) if (it < 2 || wv < 2) { ST128(ua[it], bws, uo, it * 64 * 4096, 16); ST128(ub[it], bws, uo, (it * 64 + 1) * 4096, 16); }
        }
        lds_arrive(L_CNT + 40, lane);
#undef PRD
      }
      ST(2);
      arrive_flag(L_CNT + 4, 4, p.ws, FL_U + cq * 32 + tt, tag, lane);
      ST(3);
      lds_wait<1>(L_CNT + 4, 4);
      if (UDELAY) __builtin_amdgcn_s_sleep(UDELAY);
      wait_flags(p.ws, rep + FL_U + (g >> 1) * 32 + wv * 8, 8, tag, lane);
      ST(6);
      lds_arrive(L_CNT + 28, lane); lds_wait<0>(L_CNT + 28, 4);
      dma_sc1(rws, lane * 16, WS_UT + (c5 + 4) * 4096 + wv * 1024, L_CONV5 + L_UOFF + wv * 1024);
#pragma unroll
      for (int j = 0; j < 4; ++j) dma_sc1(rws, lane * 16, WS_UT + c0 * 4096 + j * 1024, L_CONV + wv * L_CH + L_UOFF + j * 1024);
      waitv<4>();
      lds_arrive(L_CNT + 56, lane); lds_wait<0>(L_CNT + 56, 8);
      ST(4);
      conv_wave((4 - wv) * (int)L_CH + 128 * wv, ca, p.ws, wv, lane STPASS);
      ST(5);
      arrive_flag(L_CNT + 8, 4, p.ws, FL_Y + g * 16 + rt, tag, lane);
      ST(7);
      lds_wait<1>(L_CNT + 20, 4);
    } else {
      if (lw == 2) warm_x(2); else warm_w(2 + lw - (lw >> 1));
      GET_TAG();
      unsigned w1o = (lw & 1) ? off_w(5 + (lw >> 1)) : off_x(3 + (lw >> 1)), wqo;
      { const unsigned r5 = lane / 5, rho = 12 * lw + r5; int kb = k0 + (rho >> 2); kb = kb >= 10 ? kb - 10 : kb;
        wqo = (lane < 60 && rho < 40) ? (kb * 64 + iq * 4 + (rho & 3)) * 3840 + (xcd & 1) * 640 + (lane - r5 * 5) * 128 : OOB; PINV(wqo); }
      waitv<1>(); bar();
      warm((lw & 1) ? rw : rx, w1o, 0);
#pragma unroll 1
      for (int c = lw; c < 5; c += 4) { const unsigned cb = L_CONV + c * L_CH;
        if (lane < 38) sts128(lane < 30 ? cb + L_UOFF + 4096 + lane * 16 : cb + ((lane - 30) >> 1) * L_CS + (lane & 1) * 16, (v4u){0, 0, 0, 0}); }
      lds_wait<2>(L_CNT + 40, 4);
      const v4i rh = mk_rsrc(p.h, 640 * 4096);
#pragma unroll
      for (int c = 0; c < 5; ++c) dma(rh, lane * 16, (c5 + c) * 4096 + lw * 1024, L_CONV + c * L_CH + 32 + 3 * L_CS + lw * 1024);
      row_offsets(); out_offsets();
      waitv<0>();
      lds_arrive(L_CNT + 32, lane);
      lds_wait<1>(L_CNT + 4, 4);
      warm(rw, wqo, 0);
      lds_wait<1>(L_CNT + 32, 4);
      ST(14);
#pragma unroll 1
      for (int c = 4; c >= 0; --c) {
        const unsigned a = L_CONV + c * L_CH + 32 + lw * 1024 + lane * 16;
        const v4u d = lds128(a + 3 * L_CS); const v2u m = lds64(a + 3 * L_CS - 8);
        const unsigned e0 = __builtin_amdgcn_alignbit(m.y, m.x, 16), e1 = __builtin_amdgcn_alignbit(d.x, m.y, 16), e2 = __builtin_amdgcn_alignbit(d.y, d.x, 16),
                       e3 = __builtin_amdgcn_alignbit(d.z, d.y, 16), e4 = __builtin_amdgcn_alignbit(d.w, d.z, 16);
        sts128(a, (v4u){e0, e1, e2, e3}); sts128(a + L_CS, (v4u){m.y, d.x, d.y, d.z}); sts128(a + 2 * L_CS, (v4u){e1, e2, e3, e4});
        if (c == 4) lds_arrive(L_CNT + 56, lane);
      }
      lds_arrive(L_CNT + 60, lane);
      unsigned yo5 = WS_YT + cd * 4096 + 16 * l15 + 256 * (grp >> 1), wov = lane < 50 ? (iq * 400 + lw * 100 + lane) * 128 : OOB; PINV(yo5); PINV(wov);

      ST(13);
      lds_wait<2>(L_CNT + 56, 8);
      __builtin_amdgcn_s_setprio(3);
      warm(ro, wov, 0); warm(ro, wov, 6400);
      waitv<0>();
      v4u wr[25];
      {
        const __amdgpu_buffer_rsrc_t bo = __builtin_amdgcn_make_buffer_rsrc((void*)p.wo, 0, 640 * 1280, 0x00020000);
#pragma unroll
        for (int j = 0; j < 25; ++j) wr[j] = __builtin_amdgcn_raw_buffer_load_b128(bo, vo5[j % 5], ((lw ^ rot) * 5 + j / 5) * 32 * 1280 + gct * 160, 0);
      }
      SCHED();
      v4f ya, yb;
      {
        const float dv = blo(dbits);
        const unsigned ub = L_CONV5 + L_UOFF + 4088 - 32 * l15 - 8 * grp;
        const v2u ua = lds64(ub - 512 * lw), uc = lds64(ub - 512 * (7 - lw));
        ya = (v4f){mul1(dv, bhi(ua.y)), mul1(dv, blo(ua.y)), mul1(dv, bhi(ua.x)), mul1(dv, blo(ua.x))};
        yb = (v4f){mul1(dv, bhi(uc.y)), mul1(dv, blo(uc.y)), mul1(dv, bhi(uc.x)), mul1(dv, blo(uc.x))};
        PINV(ya); PINV(yb);
      }
      lds_wait<2>(L_CNT + 12, 4);
#pragma unroll
      for (int w = 0; w < 4; ++w) { ya = add4(ya, __builtin_bit_cast(v4f, lds128(L_RED + (lw * 4 + w) * 1024 + lane * 16))); yb = add4(yb, __builtin_bit_cast(v4f, lds128(L_RED + ((7 - lw) * 4 + w) * 1024 + lane * 16))); }
      conv_store(conv_pack(ya, yb), lw, yo5, p.ws, lane);
      lds_arrive(L_CNT + 12, lane); lds_wait<1>(L_CNT + 12, 8);
      ST(6);
      {
        const v4i rq = mk_rsrc(p.q, 2048 * 1280);
#pragma unroll
        for (int j = 0; j < 5; ++j) dma(rq, vo5[j], (t0 + (lw & 1) * 32) * 1280 + cq * 320 + (lw >> 1) * 160, L_QT + lw * 5120 + j * 1024);
      }
      waitv<0>();
      ST(17);
      bar();
      __builtin_amdgcn_s_setprio(0);
      unsigned wb = L_CONV + lw * 25600 + lane * 16; PINV(wb);
#pragma unroll
      for (int j = 0; j < 25; ++j) sts128(wb + j * 1024, wr[j]);
      lds_arrive(L_CNT + 20, lane);
      ST(7);
      const unsigned sta = L_QT + lw * 2560;
      v4f qf[2][5];
#pragma unroll
      for (int tl = 0; tl < 2; ++tl)
#pragma unroll
        for (int n = 0; n < 5; ++n) { const v2u u = lds64(sta + tl * 10240 + l15 * 160 + grp * 8 + n * 32); qf[tl][n] = (v4f){blo(u.x), bhi(u.x), blo(u.y), bhi(u.y)}; PINV(qf[tl][n]); }
      unsigned yo = (lane >> 3) * 4096 + (lane & 3) * 16 + (lane & 4) * 64; PINV(yo);
      unsigned yl = L_YT + (4 * grp + (l15 >> 2)) * 128 + (lane & 2) * 32 + lw * 16 + (lane & 1) * 8; PINV(yl);
      const unsigned ys = WS_YT + (160 * cq + 40 * lw) * 4096 + (t0 >> 8) * 512 + (t0 & 192);
      if (YDELAY) { lds_wait<2>(L_CNT + 8, 4); __builtin_amdgcn_s_sleep(YDELAY); }
      wait_flags(p.ws, rep + FL_Y + cq * 32 + lw * 8, 8, tag, lane);
      ST(8);
#pragma unroll
      for (int j = 0; j < 5; ++j) dma_sc1(rws, yo, ys + j * 8 * 4096, L_YT + lw * 5120 + j * 1024);
      lds_arrive(L_CNT + 52, lane);
      waitv<0>();
      lds_arrive(L_CNT + 48, lane); lds_wait<0>(L_CNT + 48, 4);
#pragma unroll
      for (int tl = 0; tl < 2; ++tl) {
        v2u res[5];
#pragma unroll
        for (int n = 0; n < 5; ++n) { const v2u u = ldtr(yl + tl * 10240 + n * 2048); res[n] = pk4(mul4(qf[tl][n], (v4f){blo(u.x), bhi(u.x), blo(u.y), bhi(u.y)})); }
        OUT_TILE(res, sta + tl * 10240, t0 * 2560 + cq * 320 + tl * 160, AUX_A);
      }
      ST(11);
    }
  }
  if (wv < 4) {
    ST(9);
    const int mt = (gr0 >> 4) + wv;
    unsigned wbq[4], wbt[4];
#pragma unroll
    for (int y = 0; y < 4; ++y) { const unsigned base = isA ? L_CONV + y * 25600 : y ? L_WQTOP - y * 25600 : 0;
      wbq[y] = base + trl; wbt[y] = base + (20 * grp + (l15 >> 2) + 16) * 160 + 8 * (lane & 3); PINV(wbq[y]); PINV(wbt[y]); }
    v4u wf[7][5];
#define WRD(t, n) do { const int y_ = (t) / 5, s_ = (t) % 5; const unsigned ra = (s_ >> 1) * 80 + (s_ & 1) * 40; \
      wf[(t) % 7][n] = s_ < 4 ? cat(ldtr(wbq[y_] + ra * 160 + (n) * 32), ldtr(wbq[y_] + (ra + 20) * 160 + (n) * 32)) \
                              : cat(ldtr(wbt[y_] + (n) * 32), ldtr(wbt[y_] + 80 * 160 + (n) * 32)); } while (0)
#pragma unroll
    for (int f = 0; f < 35; ++f) WRD(f / 5, f % 5);
    SCHED();
    if (!isA) { lds_wait<1>(L_CNT + 16, 4); if (ODELAY) __builtin_amdgcn_s_sleep(ODELAY); }
    else if (AGATE) lds_wait<1>(L_CNT + AGATE, 4);
    if (b == 0 && wv == 0) { wait_flags(p.ws, FL_U, 64, tag, lane); wait_flags(p.ws, FL_U + 64, 64, tag, lane); }
    wait_flags<OSLEEP>(p.ws, FL_O + (mt >> 1) * 8, 8, tag, lane);
    ST(10);
    v4u of[20];
#define ORD(t) do { const int s_ = (t) % 5; const unsigned oo = WS_OF + (mt * 8 + 2 * (((t) / 5) ^ rot)) * 2560; \
      if (s_ < 4) of[t] = __builtin_amdgcn_raw_buffer_load_b128(bws, lane * 16 + (s_ & 1) * 1024, oo + (s_ >> 1) * 2560, 16); \
      else of[t] = cat(__builtin_amdgcn_raw_buffer_load_b64(bws, lane * 8 + 2048, oo, 16), __builtin_amdgcn_raw_buffer_load_b64(bws, lane * 8 + 2048, oo + 2560, 16)); } while (0)
#pragma unroll
    for (int t = 0; t < OAHEAD; ++t) { ORD(t); SCHED(); }
    if (b == 0 && tid == 0) __hip_atomic_store((GAS u64*)p.ws, tag, RLX_AGENT);
    SCHED();
    v4f acc[5];
#pragma unroll
    for (int n = 0; n < 5; ++n) acc[n] = (v4f){0, 0, 0, 0};
#pragma unroll
    for (int t = 0; t < 20; ++t) {
      v4u cur[5];
#pragma unroll
      for (int n = 0; n < 5; ++n) cur[n] = wf[t % 7][n];
#pragma unroll
      for (int f = 0; f < 65; ++f) if (t >= 1 && f >= (t - 1) * 15 / 4 && f < t * 15 / 4) WRD(7 + f / 5, f % 5);
#pragma unroll
      for (int n = 0; n < 5; ++n) acc[n] = mfma(cur[n], of[t], acc[n]);
      if (t + OAHEAD < 20) ORD(t + OAHEAD);
      __builtin_amdgcn_sched_group_barrier(0x8, 1, 0); __builtin_amdgcn_sched_group_barrier(0x100, 2, 0);
      __builtin_amdgcn_sched_group_barrier(0x8, 1, 0); __builtin_amdgcn_sched_group_barrier(0x100, 1, 0); __builtin_amdgcn_sched_group_barrier(0x20, 1, 0);
      __builtin_amdgcn_sched_group_barrier(0x8, 1, 0); __builtin_amdgcn_sched_group_barrier(0x100, 2, 0);
      __builtin_amdgcn_sched_group_barrier(0x8, 1, 0); __builtin_amdgcn_sched_group_barrier(0x100, 1, 0); __builtin_amdgcn_sched_group_barrier(0x20, 1, 0);
      __builtin_amdgcn_sched_group_barrier(0x8, 1, 0); __builtin_amdgcn_sched_group_barrier(0x100, 2, 0);
      SCHED();
    }
#undef WRD
#undef ORD
    v2u res[5];
#pragma unroll
    for (int n = 0; n < 5; ++n) res[n] = pk4(acc[n]);
    const unsigned stb = (isA ? L_STA : L_STB) + wv * 2560;
    OUT_TILE(res, stb, gr0 * 2560 + 1280 + gct * 160, AUX_B);
    ST(11);
  }
}

extern "C" void kernel_launch(void* const* d_in, const int* in_sizes, int n_in, void* d_out, int out_size, void* d_ws, size_t ws_size, hipStream_t stream) {
  Params p;
  p.q = (const u16*)d_in[0]; p.k = (const u16*)d_in[1]; p.v = (const u16*)d_in[2]; p.sk = (const u16*)d_in[3]; p.h = (const u16*)d_in[4];
  p.Dv = (const u16*)d_in[5]; p.x = (const u16*)d_in[6]; p.w = (const u16*)d_in[7]; p.wo = (const u16*)d_in[8];
  p.out = (u16*)d_out; p.ws = (char*)d_ws;
  fused_kernel<<<256, 512, 0, stream>>>(p);
}
```

```cpp
#include <hip/hip_runtime.h>
typedef unsigned short u16;
typedef unsigned long long u64;
typedef unsigned v2u __attribute__((ext_vector_type(2)));
typedef unsigned v4u __attribute__((ext_vector_type(4)));
typedef int v4i __attribute__((ext_vector_type(4)));
typedef float v2f __attribute__((ext_vector_type(2)));
typedef float v4f __attribute__((ext_vector_type(4)));
typedef short v4s __attribute__((ext_vector_type(4)));
typedef __bf16 v2bf __attribute__((ext_vector_type(2)));
typedef __bf16 v8bf __attribute__((ext_vector_type(8)));
#define GAS __attribute__((address_space(1)))
#define LAS __attribute__((address_space(3)))
#define RLX_AGENT __ATOMIC_RELAXED, __HIP_MEMORY_SCOPE_AGENT
#define DEVINL __device__ __forceinline__

constexpr unsigned WS_FLAG = 4096;
constexpr unsigned FL_U = 0, FL_P = 128, FL_P2 = 256, FL_Y = 384, FL_O = 512;
constexpr unsigned FL_N = 1024;
constexpr unsigned WS_UT = 2u << 20, WS_YT = 5u << 20, WS_PART = 8u << 20, WS_OF = 9u << 20, WS_STAMP = 12u << 20;
constexpr unsigned WS_BYTES = 13u << 20;
constexpr unsigned PART_STRIDE = 3584;
constexpr unsigned OOB = 0x80000000u;
constexpr unsigned L_DUMMY = 162816, L_CNT = 163584;
constexpr unsigned L_KVSLOT = 36992, L_WQTOP = 4 * L_KVSLOT, L_PC = 148480, L_KVC = 152576, L_TAB = 156672, L_STB = 46080, L_QTB = 25600, L_YTB = 56320;
constexpr unsigned L_CS = 4160, L_UOFF = 4 * L_CS, L_CH = 21248;
constexpr unsigned L_KIMG = 0, L_VIMG = 9 * 2576, L_SW = 44032, L_RED = 0, L_QT = 0, L_YT = 20480, L_STA = 40960, L_CONV = 51200, L_CONV5 = L_CONV + 4 * L_CH;
#define CODE_BYTES 47000

struct Params { const u16 *q, *k, *v, *sk, *h, *Dv, *x, *w, *wo; u16* out; char* ws; };

DEVINL v4i mk_rsrc(const void* p, unsigned bytes) {
  u64 a = (u64)p; v4i r; r.x = (int)(unsigned)a; r.y = (int)((unsigned)(a >> 32) & 0xffffu); r.z = (int)bytes; r.w = 0x00020000; return r;
}
DEVINL void dma(v4i rs, unsigned voff, unsigned soff, unsigned lds) {
  asm volatile("s_mov_b32 m0, %3\n\ts_nop 0\n\tbuffer_load_dwordx4 %0, %1, %2 offen lds" :: "v"(voff), "s"(rs), "s"(soff), "s"(lds) : "memory");
}
DEVINL void dma_sc1(v4i rs, unsigned voff, unsigned soff, unsigned lds) {
  asm volatile("s_mov_b32 m0, %3\n\ts_nop 0\n\tbuffer_load_dwordx4 %0, %1, %2 offen sc1 lds" :: "v"(voff), "s"(rs), "s"(soff), "s"(lds) : "memory");
}
DEVINL void warm(v4i rs, unsigned voff, unsigned soff) {
  asm volatile("s_mov_b32 m0, %3\n\ts_nop 0\n\tbuffer_load_dword %0, %1, %2 offen lds" :: "v"(voff), "s"(rs), "s"(soff), "s"(L_DUMMY) : "memory");
}
template <int N> DEVINL void waitv() { asm volatile("s_waitcnt vmcnt(%0)" :: "n"(N) : "memory"); }
DEVINL void rawbar() { asm volatile("s_barrier" ::: "memory"); }
DEVINL void bar() { asm volatile("s_waitcnt lgkmcnt(0)\n\ts_barrier" ::: "memory"); }
#define SCHED() __builtin_amdgcn_sched_barrier(0)
DEVINL v4u lds128(unsigned a) { return *(const LAS v4u*)(size_t)a; }
DEVINL v2u lds64(unsigned a) { return *(const LAS v2u*)(size_t)a; }
DEVINL unsigned lds32(unsigned a) { return *(const LAS unsigned*)(size_t)a; }
DEVINL void sts128(unsigned a, v4u v) { *(LAS v4u*)(size_t)a = v; }
DEVINL void sts64(unsigned a, v2u v) { *(LAS v2u*)(size_t)a = v; }
DEVINL void sts32(unsigned a, unsigned v) { *(LAS unsigned*)(size_t)a = v; }
DEVINL v2u ldtr(unsigned a) { return __builtin_bit_cast(v2u, __builtin_amdgcn_ds_read_tr16_b64_v4i16((LAS v4s*)(size_t)a)); }
DEVINL v4u cat(v2u a, v2u b) { return (v4u){a.x, a.y, b.x, b.y}; }
DEVINL v4f mfma(v4u a, v4u b, v4f c) { return __builtin_amdgcn_mfma_f32_16x16x32_bf16(__builtin_bit_cast(v8bf, a), __builtin_bit_cast(v8bf, b), c, 0, 0, 0); }
DEVINL unsigned pk2(float a, float b) { v2f p = {a, b}; return __builtin_bit_cast(unsigned, __builtin_convertvector(p, v2bf)); }
DEVINL v2u pk4(v4f a) { return (v2u){pk2(a[0], a[1]), pk2(a[2], a[3])}; }
DEVINL float blo(unsigned u) { return __uint_as_float(u << 16); }
DEVINL float bhi(unsigned u) { return __uint_as_float(u & 0xffff0000u); }
DEVINL v2u phi4s(v4f c) {
  float r[4];
#pragma unroll
  for (int j = 0; j < 4; ++j) {
    r[j] = __int_as_float(max(__float_as_int(c[j]), 0x358637bd)) + __builtin_amdgcn_fmed3f(__builtin_amdgcn_exp2f(__builtin_fmaf(c[j], 1.44269504089f, -1.44269504089e-6f)), 0.f, 1.f);
    asm("" : "+v"(r[j]));
  }
  return (v2u){pk2(r[0], r[1]), pk2(r[2], r[3])};
}
DEVINL v2u phi4(v4f c) {
  const v4f a = c * 1.44269504089f - 1.44269504089e-6f;
  v4f r;
#pragma unroll
  for (int j = 0; j < 4; ++j) r[j] = __int_as_float(max(__float_as_int(c[j]), 0x358637bd));
  const v4f e = {__builtin_amdgcn_fmed3f(__builtin_amdgcn_exp2f(a[0]), 0.f, 1.f), __builtin_amdgcn_fmed3f(__builtin_amdgcn_exp2f(a[1]), 0.f, 1.f),
                 __builtin_amdgcn_fmed3f(__builtin_amdgcn_exp2f(a[2]), 0.f, 1.f), __builtin_amdgcn_fmed3f(__builtin_amdgcn_exp2f(a[3]), 0.f, 1.f)};
  return pk4(r + e);
}
DEVINL float add1(float a, float b) { float r; asm("v_add_f32 %0, %1, %2" : "=v"(r) : "v"(a), "v"(b)); return r; }
DEVINL float mul1(float a, float b) { float r; asm("v_mul_f32 %0, %1, %2" : "=v"(r) : "v"(a), "v"(b)); return r; }
DEVINL v4f add4(v4f a, v4f b) { return (v4f){add1(a[0], b[0]), add1(a[1], b[1]), add1(a[2], b[2]), add1(a[3], b[3])}; }
DEVINL v4f mul4(v4f a, v4f b) { return (v4f){mul1(a[0], b[0]), mul1(a[1], b[1]), mul1(a[2], b[2]), mul1(a[3], b[3])}; }
DEVINL float dot2(unsigned a, unsigned b, float c) { return __builtin_amdgcn_fdot2_f32_bf16(__builtin_bit_cast(v2bf, a), __builtin_bit_cast(v2bf, b), c, false); }
DEVINL float dot0(unsigned a, unsigned b) { float r; asm("v_dot2_f32_bf16 %0, %1, %2, 0" : "=v"(r) : "v"(a), "v"(b)); return r; }
#define ST128(v, rs, voff, soff, aux) __builtin_amdgcn_raw_buffer_store_b128(v, rs, (voff) + (soff), 0, aux)
#define PINV(x) asm volatile("" : "+v"(x))
#define PINS(x) asm volatile("" : "+s"(x))

DEVINL void set_flag(char* ws, unsigned idx, u64 tag, int nrep, int lane) {
  const __amdgpu_buffer_rsrc_t rs = __builtin_amdgcn_make_buffer_rsrc(ws, 0, WS_BYTES, 0x00020000);
  const unsigned lo = (unsigned)tag, hi = (unsigned)(tag >> 32);
  if (lane < 8 * nrep) ST128(((v4u){lo, hi, lo, hi}), rs, (lane >> 3) * (FL_N * 128) + (lane & 7) * 16, WS_FLAG + idx * 128, 16);
}
#define NREP 8
#define UDELAY 18
#define YDELAY 8
#define ODELAY 6
#define PDELAY 0
#define AGATE 52
#define OREP 1
#define OSLEEP 2
#define XPOS 4
#define PHINT 0
#define QEARLY 2
#define PHIK phi4
#define EPRIO 2
#define USTORE 1
#define NPRE 2
#define WSPLIT 1
#define YPOS 0
#define ACNT 4
#define ADELAY 0
#define WLATE 0
#define PPRIO 0
#define AWARM 1
#define CWALL 1
#define RING 9
#define PACE (22 - RING)
#define BGATE 0
#define QPRE 0
#define PHIQ phi4
#define SUMGATE 0
#define QPRIO 0
#define WFIRST 0
#define AUX_A 2
#define AUX_B 0
#define KVDELAY 16
#define OAHEAD 8
#define PSLEEP 2
template <int SL = PSLEEP> DEVINL void wait_flags(char* ws, unsigned idx, int n, u64 tag, int lane) {
  if (lane < n) {
    GAS u64* p = (GAS u64*)(ws + WS_FLAG + (idx + lane) * 128u);
#pragma unroll 1
    for (unsigned spins = 0; spins < (1u << 16); ++spins) {
      if (__hip_atomic_load(p, RLX_AGENT) == tag) break;
      __builtin_amdgcn_s_sleep(SL);
    }
  }
  __builtin_amdgcn_fence(__ATOMIC_ACQUIRE, "wavefront");
  asm volatile("" ::: "memory");
}
DEVINL unsigned cnt_addr(unsigned cnt, int lane) { return lane ? L_DUMMY + 320 + lane * 4 : cnt; }
template <bool DRAIN> DEVINL void arrive_set(unsigned cnt, unsigned n, char* ws, unsigned idx, u64 tag, int lane) {
  unsigned old;
  if (DRAIN) waitv<0>();
  asm volatile("ds_add_rtn_u32 %0, %1, %2\n\ts_waitcnt lgkmcnt(0)" : "=&v"(old) : "v"(cnt_addr(cnt, lane)), "v"(1u) : "memory");
  if (__builtin_amdgcn_readfirstlane(old) == n - 1) set_flag(ws, idx, tag, NREP, lane);
}
#define arrive_flag arrive_set<true>
#define arrive_hint arrive_set<false>
DEVINL void lds_arrive(unsigned cnt, int lane) {
  asm volatile("ds_add_u32 %0, %1" :: "v"(cnt_addr(cnt, lane)), "v"(1u) : "memory");
}
template <int SLEEP> DEVINL void lds_wait(unsigned cnt, unsigned n) {
  unsigned v, sv, spins;
  asm volatile("s_mov_b32 %2, 0x100000\n1:\n\tds_read_b32 %0, %3\n\ts_waitcnt lgkmcnt(0)\n\tv_readfirstlane_b32 %1, %0\n\ts_cmp_ge_u32 %1, %4\n\ts_cbranch_scc1 2f\n\t"
               "s_sleep %5\n\ts_sub_u32 %2, %2, 1\n\ts_cbranch_scc0 1b\n2:"
               : "=&v"(v), "=&s"(sv), "=&s"(spins) : "v"(cnt), "s"(n), "n"(SLEEP) : "memory", "scc");
}
#define ST(i)
#define STC(i)
#define STARG
#define STPASS

DEVINL void gemm_kv(v4f (&aK)[8], v4f (&aV)[8], v4f (&aT)[4], unsigned xa, unsigned xb, unsigned wa, unsigned wt STARG) {
  v4u xp[8], wp[3], xq[8], wq[3];
  unsigned so = L_KVSLOT;
#define RD(X, W, hs) do { X[0] = lds128((xa + so) ^ ((hs) * 64)); \
    W[0] = cat(ldtr(wa + so + (hs) * 5120), ldtr(wa + so + (hs) * 5120 + 2560)); \
    W[1] = cat(ldtr(wa + so + (hs) * 5120 + 10256), ldtr(wa + so + (hs) * 5120 + 2560 + 10256)); \
    W[2] = cat(ldtr(wt + so + (hs) * 5120), ldtr(wt + so + (hs) * 5120 + 2560)); \
    _Pragma("unroll") for (int m = 1; m < 8; ++m) X[m] = lds128((((m < 4 ? xa : xb) + so) ^ ((hs) * 64)) + (m & 3) * 2048); } while (0)
#define MM(X, W) do { _Pragma("unroll") for (int m = 0; m < 8; ++m) { aK[m] = mfma(X[m], W[0], aK[m]); aV[m] = mfma(X[m], W[1], aV[m]); if (m < 4) aT[m] = mfma(X[m], W[2], aT[m]); } } while (0)
#define MIX() do { _Pragma("unroll") for (int i = 0; i < 20; ++i) { __builtin_amdgcn_sched_group_barrier(0x8, 1, 0); __builtin_amdgcn_sched_group_barrier(0x100, 1, 0); } } while (0)
  bar(); SCHED();
  RD(xp, wp, 0);
  SCHED();
#pragma unroll 1
  for (int s = 0; s < 9; ++s) {
    RD(xq, wq, 1); MM(xp, wp); MIX(); SCHED();
    bar(); SCHED();
    so = (so == 0) ? 3 * L_KVSLOT : so - L_KVSLOT;
    RD(xp, wp, 0); MM(xq, wq); MIX(); SCHED();
  }
  RD(xq, wq, 1); MM(xp, wp); MIX(); SCHED();
  ST(1);
#pragma unroll
  for (int m = 0; m < 4; ++m) aT[m] = mfma(xq[m], wq[2], aT[m]);
#pragma unroll
  for (int m = 0; m < 8; ++m) aK[m] = mfma(xq[m], wq[0], aK[m]);
#pragma unroll
  for (int m = 0; m < 8; ++m) aV[m] = mfma(xq[m], wq[1], aV[m]);
#undef RD
#undef MM
#undef MIX
}
struct ConvArgs { unsigned ha, ua, yo; v4u dg; };
DEVINL ConvArgs conv_args(unsigned cb, unsigned dbits, unsigned yoff, int lane) {
  const int i = lane & 15, g = lane >> 4, jt = 15 - i - 8 * g;
  ConvArgs c;
  c.ha = cb + (i >> 2) * L_CS + 16 * g + 8 * (i & 3) + 8;
  c.ua = cb + L_UOFF + 480 - 32 * i + 16 * g;
  c.yo = yoff + 16 * i + 256 * (g >> 1);
#pragma unroll
  for (int d = 0; d < 4; ++d) c.dg[d] = (jt == 2 * d) ? dbits : (jt == 2 * d + 1) ? (dbits << 16) : 0u;
  PINV(c.ha); PINV(c.ua); PINV(c.yo); PINV(c.dg);
  return c;
}
DEVINL v4u conv_pack(v4f a, v4f b) {
  v2u X = pk4(a), Y = pk4(b);
  auto r0 = __builtin_amdgcn_permlane16_swap(X.x, Y.x, false, false);
  auto r1 = __builtin_amdgcn_permlane16_swap(X.y, Y.y, false, false);
  return (v4u){r0[0], r1[0], r0[1], r1[1]};
}
DEVINL void conv_store(v4u o, int ta, unsigned yo, char* ws, int lane) {
  const __amdgpu_buffer_rsrc_t rs = __builtin_amdgcn_make_buffer_rsrc(ws, 0, WS_BYTES, 0x00020000);
  ST128(o, rs, yo + ((lane & 16) ? 7 - ta : ta) * 512, 0, 16);
}
DEVINL void conv_wave(int off5, ConvArgs c, char* ws, int wv, int lane STARG) {
  unsigned ha = c.ha + off5, ua = c.ua + off5;
  v4f acc[8], ac5[8];
  v4u F[8], G[8], F2[8], G2[8];
#define CRD(F, G) do { F[0] = cat(ldtr(ha), ldtr(ha + 8)); _Pragma("unroll") for (int v = 0; v < 8; ++v) G[v] = lds128(ua + (7 - v) * 512); \
    _Pragma("unroll") for (int v = 1; v < 8; ++v) F[v] = cat(ldtr(ha + v * 512), ldtr(ha + v * 512 + 8)); ha += 64; ua += 64; PINV(ha); PINV(ua); } while (0)
#define CMM(A, F, G) do { _Pragma("unroll") for (int w = 0; w < 8; ++w) _Pragma("unroll") for (int t = w; t < 8; ++t) A[t] = mfma(F[w], G[t - w], A[t]); } while (0)
#define CMIX() do { _Pragma("unroll") for (int j = 0; j < 36; ++j) { __builtin_amdgcn_sched_group_barrier(0x8, 1, 0); __builtin_amdgcn_sched_group_barrier(0x100, 1, 0); } } while (0)
  CRD(F, G);
  waitv<0>(); lds_arrive(L_CNT + 60, lane);
  SCHED();
#pragma unroll
  for (int t = 0; t < 8; ++t) ac5[t] = (v4f){0, 0, 0, 0};
  CRD(F2, G2); CMM(ac5, F, G); CMIX(); SCHED();
  lds_wait<0>(L_CNT + 60, 8);
  ha = c.ha; ua = c.ua;
  CRD(F, G); CMM(ac5, F2, G2); CMIX(); SCHED();
  {
    unsigned ra = L_RED + wv * 1024 + lane * 16;
#pragma unroll
    for (int t = 0; t < 8; ++t) acc[t] = mfma(c.dg, G[t], (v4f){0, 0, 0, 0});
    CRD(F2, G2); CMM(acc, F, G);
#pragma unroll
    for (int t = 0; t < 8; ++t) sts128(ra + t * 4096, __builtin_bit_cast(v4u, ac5[t]));
#pragma unroll
    for (int j = 0; j < 24; ++j) { __builtin_amdgcn_sched_group_barrier(0x8, 1, 0); __builtin_amdgcn_sched_group_barrier(0x100, 1, 0); }
#pragma unroll
    for (int j = 0; j < 8; ++j) { __builtin_amdgcn_sched_group_barrier(0x8, 2, 0); __builtin_amdgcn_sched_group_barrier(0x200, 1, 0); }
    SCHED();
    lds_arrive(L_CNT + 12, lane);
  }
  ST(16);
#pragma unroll 1
  for (int it = 0; it < 3; ++it) {
    CRD(F, G); CMM(acc, F2, G2); CMIX(); SCHED();
    CRD(F2, G2); CMM(acc, F, G); CMIX(); SCHED();
  }
  ST(17);
  bar(); SCHED();
#undef CRD
#undef CMM
#undef CMIX
#pragma unroll
  for (int a = 0; a < 4; ++a) {
    const int b = 7 - a;
#pragma unroll
    for (int w = 0; w <= b; ++w) { acc[b] = mfma(F2[w], G2[b - w], acc[b]); if (w <= a) acc[a] = mfma(F2[w], G2[a - w], acc[a]); }
  }
#pragma unroll
  for (int a = 0; a < 4; ++a) conv_store(conv_pack(acc[a], acc[7 - a]), a, c.yo, ws, lane);
  __builtin_amdgcn_sched_group_barrier(0x8, 12, 0);
#pragma unroll
  for (int a = 0; a < 3; ++a) {
#pragma unroll
    for (int j = 0; j < 8; ++j) { __builtin_amdgcn_sched_group_barrier(0x2, 1, 0); __builtin_amdgcn_sched_group_barrier(0x8, 1, 0); }
    __builtin_amdgcn_sched_group_barrier(0x40, 1, 0);
  }
}

__global__ void __launch_bounds__(512, 2) fused_kernel(Params p) {
  __shared__ __attribute__((aligned(1024))) char smem[163840];
  u64 pc; asm volatile("s_getpc_b64 %0" : "=s"(pc));
  const int tid = threadIdx.x, lane = tid & 63;
  const int b = blockIdx.x, xcd = b & 7, bi = b >> 3;
  warm(mk_rsrc((const void*)pc, CODE_BYTES), (tid < 48 && (CWALL || bi >= 16)) ? ((bi & 7) * 48 + tid) * 128 : OOB, 0);
  const int wv = __builtin_amdgcn_readfirstlane(tid >> 6);
  const bool isA = bi >= 16;
  const int rt = 4 * (xcd >> 1) + (bi & 3), g = 4 * (xcd & 1) + ((bi >> 2) & 3);
  ST(0);
  typedef __attribute__((address_space(4))) const u64 cu64;
  typedef __attribute__((address_space(4))) const unsigned cu32;
  const int lw = wv & 3, iq = bi & 15;
  int k0 = ((xcd >> 1) * 5 >> 1) + 5 * (xcd & 1); k0 = k0 >= 10 ? k0 - 10 : k0;
  v4i rx, rw;
#define MK_XW() do { const u16 *x_ = p.x, *w_ = p.w; PINS(x_); PINS(w_); rx = mk_rsrc(x_, 2048 * 1280); rw = mk_rsrc(w_, 640 * 3840); } while (0)
  auto off_x = [&](int j) {
    int ka = k0 + 2 * j + (lane >> 5); ka = ka >= 10 ? ka - 10 : ka;
    unsigned vo = ((xcd >> 1) * 512 + iq * 32 + (lane & 31)) * 1280 + ka * 128; PINV(vo);
    return vo;
  };
  auto off_w = [&](int j) {
    const unsigned r10 = lane / 10, c10 = lane - r10 * 10, rho = 6 * j + r10; int kb = k0 + (rho >> 2); kb = kb >= 10 ? kb - 10 : kb;
    unsigned vo = (kb * 64 + iq * 4 + (rho & 3)) * 3840 + 1280 + (xcd & 1) * 640 + c10 * 128 + (c10 >= 5 ? 640 : 0); PINV(vo);
    vo = (lane < 60 && rho < 40) ? vo : OOB; PINV(vo);
    return vo;
  };
  auto warm_x = [&](int j) { warm(rx, off_x(j), 0); };
  auto warm_w = [&](int j) { warm(rw, off_w(j), 0); };
  unsigned xo0 = 0, xo1 = 0, wo_[5] = {0, 0, 0, 0, 0};
  auto loader_offsets = [&]() {
    const unsigned r = lane >> 3;
    xo0 = (rt * 128 + lw * 32 + r) * 1280 + (((lane & 7) ^ (r >> 1)) << 4);
    xo1 = (xo0 ^ 64) + 8 * 1280;
#pragma unroll
    for (int j = 0; j < 5; ++j) { const unsigned ch = j * 64 + lane, rho = ch / 10, c = ch - rho * 10;
      wo_[j] = (8 * ((rho >> 2) & 3) + 4 * (rho >> 4) + (rho & 3)) * 3840 + c * 16; PINV(wo_[j]); }
    PINV(xo0); PINV(xo1);
  };
  const unsigned kwcol = (640 + 640 * (lw & 1) + 80 * g) * 2 + (lw >> 1) * 32 * 3840, kwl = 16384 + (lw & 1) * 10256 + (lw >> 1) * 5120;
  auto issue_x = [&](int ks, unsigned so) {
#pragma unroll
    for (int j = 0; j < 4; ++j) dma(rx, (j & 1) ? xo1 : xo0, ks * 128 + (j >> 1) * 16 * 1280, so + lw * 4096 + j * 1024);
  };
  auto issue_kw = [&](int ks, unsigned so) {
#pragma unroll
    for (int j = 0; j < 5; ++j) dma(rw, wo_[j], kwcol + ks * 64 * 3840, so + kwl + j * 1024);
  };
  const int tt = 4 * xcd + (iq >> 2), cq = iq & 3, t0 = tt * 64;
  if (isA) {
    const unsigned r20 = lane / 20, so = (t0 + wv * 8) * 1280, la = L_KIMG + 2576 + wv * 2576;
    unsigned vo = r20 * 1280 + (lane - r20 * 20) * 16 + cq * 320; PINV(vo);
    if (AWARM) {
      const bool wx = wv == 0 || wv == 3; const unsigned xv = off_x(wv & 1), wz = off_w(wv - 1);
      MK_XW();
      warm(wx ? rx : rw, wv > 3 ? OOB : wx ? xv : wz, 0);
    } else { const unsigned xv = off_x(1); MK_XW(); if (wv == 0) warm(rx, xv, 0); }
    if (KVDELAY) __builtin_amdgcn_s_sleep(KVDELAY);
    const v4i rk = mk_rsrc(p.k, 2048 * 1280), rv = mk_rsrc(p.v, 2048 * 1280);
    if (lane < 60) {
      dma(rk, vo, so, la); dma(rk, vo, so + 3840, la + 960); dma(rv, vo, so, la + 8 * 2576); dma(rv, vo, so + 3840, la + 8 * 2576 + 960);
      if (wv == 7) dma(rk, tt ? vo + (t0 - 3) * 1280 : OOB, 0, L_KIMG + 1600);
      if (lane < 40) {
        dma(rk, vo, so + 7680, la + 1920); dma(rv, vo, so + 7680, la + 8 * 2576 + 1920);
        if (wv == 5 || wv == 6) dma(mk_rsrc(p.sk, 640 * 8), cq * 1280 + (wv - 5) * 640 + lane * 16, 0, L_SW + (wv - 5) * 640);
      }
    }
  } else if (wv >= 4) { loader_offsets(); MK_XW(); issue_x(k0, L_KVSLOT); issue_kw(k0, L_KVSLOT); }
  else if (AWARM) { MK_XW();
    if (PHINT) ST128(((v4u){~0u, ~0u, ~0u, ~0u}), __builtin_amdgcn_make_buffer_rsrc(p.ws, 0, WS_BYTES, 0x00020000), lane < 56 ? lane * 16 : OOB, WS_PART + (g * 16 + rt) * PART_STRIDE + wv * 896, 16); }
  else { const unsigned xv = off_x(0), wz = off_w(wv - 1); MK_XW(); warm(wv == 0 ? rx : rw, wv == 0 ? xv : wv < 3 ? wz : OOB, 0);
    if (PHINT) ST128(((v4u){~0u, ~0u, ~0u, ~0u}), __builtin_amdgcn_make_buffer_rsrc(p.ws, 0, WS_BYTES, 0x00020000),
                                                      lane < 56 ? lane * 16 : OOB, WS_PART + (g * 16 + rt) * PART_STRIDE + wv * 896, 16);
  }
  ST(12);
  asm volatile("" ::: "memory");
  const int c5 = 80 * g + 5 * rt, c0 = c5 + lw, cd = wv < 4 ? c0 : c5 + 4;
  const v4i rws = mk_rsrc(p.ws, WS_BYTES), ro = mk_rsrc(p.wo, 640 * 1280);
  const __amdgpu_buffer_rsrc_t bws = __builtin_amdgcn_make_buffer_rsrc(p.ws, 0, WS_BYTES, 0x00020000);
  const __amdgpu_buffer_rsrc_t bout = __builtin_amdgcn_make_buffer_rsrc(p.out, 0, 2048 * 2560, 0x00020000);
  const unsigned rep = (xcd & (NREP - 1)) * FL_N;
  const int l15 = lane & 15, grp = lane >> 4;
  const unsigned trr = (4 * grp + (l15 >> 2)) * 160, trl = trr + 8 * (lane & 3);
  const int gr0 = (4 * xcd + ((bi >> 2) & 3)) * 64, gct = (bi & 3) + 4 * (bi >> 4), rot = bi & 3;
  unsigned vo5[5];
  auto row_offsets = [&]() {
#pragma unroll
    for (int j = 0; j < 5; ++j) { const unsigned ch = j * 64 + lane, r = ch / 10, c = ch - r * 10; vo5[j] = r * 1280 + c * 16; PINV(vo5[j]); }
  };
  unsigned oo3[3];
  auto out_offsets = [&]() {
#pragma unroll
    for (int j = 0; j < 3; ++j) { const unsigned ch = j * 64 + lane, r = ch / 10; oo3[j] = ch < 160 ? (lw * 16 + r) * 2560 + (ch - r * 10) * 16 : OOB; PINV(oo3[j]); }
  };
#define OUT_TILE(res, sta, soff, aux) do { const unsigned w_ = (sta) + l15 * 160 + grp * 8; \
    _Pragma("unroll") for (int n = 0; n < 5; ++n) sts64(w_ + n * 32, res[n]); \
    asm volatile("" ::: "memory"); \
    _Pragma("unroll") for (int j = 0; j < 3; ++j) ST128(lds128((sta) + j * 1024 + lane * 16), bout, oo3[j], soff, aux); } while (0)
  u64 tag; unsigned dbits;
#define GET_TAG() do { const char* w_ = p.ws; const u16* d_ = p.Dv; PINS(w_); PINS(d_); u64 e_ = *(cu64*)w_; dbits = ((cu32*)d_)[cd >> 1]; PINS(e_); PINS(dbits); \
    dbits = (dbits >> (16 * (cd & 1))) & 0xffffu; tag = e_ * 6364136223846793005ull + 1442695040888963407ull; PINS(tag); } while (0)
  auto gate = [&](unsigned lq, unsigned ly, int tl) {
    const unsigned sta = lq + lw * 2560;
    v4f qf[5];
#pragma unroll
    for (int n = 0; n < 5; ++n) { const v2u u = lds64(sta + l15 * 160 + grp * 8 + n * 32); qf[n] = (v4f){blo(u.x), bhi(u.x), blo(u.y), bhi(u.y)}; PINV(qf[n]); }
    unsigned yo = (lane >> 3) * 4096 + (lane & 3) * 16 + (lane & 4) * 64; PINV(yo);
    unsigned yl = ly + (4 * grp + (l15 >> 2)) * 128 + (lane & 2) * 32 + lw * 16 + (lane & 1) * 8; PINV(yl);
    const unsigned ys = WS_YT + (160 * cq + 80 * tl + 20 * lw) * 4096 + (t0 >> 8) * 512 + (t0 & 192);
    if (tl == 0) { lds_wait<2>(L_CNT + 8, 4); __builtin_amdgcn_s_sleep(YDELAY); }
    wait_flags(p.ws, rep + FL_Y + cq * 32 + tl * 16 + lw * 4, 4, tag, lane);
    if (tl == 0) ST(8); else ST(10);
    dma_sc1(rws, yo, ys, ly + lw * 2560); dma_sc1(rws, yo, ys + 8 * 4096, ly + lw * 2560 + 1024);
    if (lane < 32) dma_sc1(rws, yo, ys + 16 * 4096, ly + lw * 2560 + 2048);
    lds_arrive(L_CNT + 52, lane);
    waitv<0>();
    lds_arrive(L_CNT + 64, lane); lds_wait<0>(L_CNT + 64, 4);
    v2u res[5];
#pragma unroll
    for (int n = 0; n < 5; ++n) { const v2u u = ldtr(yl + n * 2048); res[n] = pk4(mul4(qf[n], (v4f){blo(u.x), bhi(u.x), blo(u.y), bhi(u.y)})); }
    OUT_TILE(res, sta, t0 * 2560 + cq * 320 + tl * 160, AUX_A);
  };
  if (tid == 0) { asm volatile("" :: "v"(smem)); sts64(L_CNT + 64, (v2u){0, 0}); sts128(L_CNT, (v4u){0, 0, 0, 0}); sts128(L_CNT + 16, (v4u){0, 0, 0, 0}); sts128(L_CNT + 32, (v4u){0, 0, 0, 0}); sts128(L_CNT + 48, (v4u){0, 0, 0, 0}); }

  if (!isA) {
    unsigned vw0, vw4;
    auto wo_offsets = [&]() { unsigned l_ = lane; PINV(l_); const unsigned c0_ = lw * 64 + l_, r0 = c0_ / 10, c4 = 256 + l_, r4 = c4 / 10;
      vw0 = r0 * 1280 + (c0_ - r0 * 10) * 16; vw4 = r4 * 1280 + (c4 - r4 * 10) * 16; PINV(vw0); PINV(vw4); };
    auto load_wo = [&](int y, unsigned base) {
      int gb = gct; PINS(gb);
      dma(ro, vw4, ((y ^ rot) * 5 + lw) * 32 * 1280 + gb * 160, base + lw * 5120 + 4096);
      if (lw == y) dma(ro, vw4, ((y ^ rot) * 5 + 4) * 32 * 1280 + gb * 160, base + 4 * 5120 + 4096);
#pragma unroll
      for (int i = 0; i < 5; ++i) dma(ro, vw0, ((y ^ rot) * 5 + i) * 32 * 1280 + gb * 160, base + i * 5120 + lw * 1024);
    };
    if (wv < 4) {
      GET_TAG();
      const int hf = wv & 1;
      const bool isk = !(l15 & 4), own = (l15 >> 2) == 2 * hf + 1;
      const unsigned pc0 = L_PC + wv * 896, trash = L_DUMMY + 256;
      const int jb = own ? (l15 & 3) : l15 == 0 ? 4 : -1;
      unsigned pb = jb >= 0 ? pc0 + (80 + 5 * grp + jb) * 8 : trash, pcc = grp == 2 * hf ? pc0 + (64 + l15) * 8 : trash,
               pd = (grp == 2 * hf && jb >= 0) ? pc0 + (100 + jb) * 8 : trash;
      PINV(pb); PINV(pcc); PINV(pd);
      out_offsets(); wo_offsets();
      {
        const unsigned c_ = l15 >> 2, r_ = l15 & 3, zu = L_KVC + 3584;
        const unsigned sel = wv == 0 ? grp : wv == 1 ? c_ : r_;
        const unsigned a = L_KVC + sel * 896 + (wv == 0 ? 64 + l15 : wv == 1 ? 80 + 5 * grp + r_ : 84 + 5 * grp) * 8;
        v4u t = {sel == 0 ? a : zu, sel == 1 ? a : zu, sel == 2 ? a : zu, sel == 3 ? a : zu};
        if (wv == 3) t = (v4u){(unsigned)grp == c_ ? L_KVC + c_ * 896 + (100 + r_) * 8 : zu, (unsigned)grp == r_ ? L_KVC + r_ * 896 + 104 * 8 : zu,
                               (unsigned)((grp & 1) * 20480 + 2048 + (grp >> 1) * 256 + l15 * 16), 0};
        sts128(L_TAB + wv * 1024 + lane * 16, t);
      }
      unsigned po = (tid < 224) ? WS_PART + g * 16 * PART_STRIDE + tid * 16 : OOB, pso = lane < 56 ? WS_PART + (g * 16 + rt) * PART_STRIDE + wv * 896 + lane * 16 : OOB;
      PINV(po); PINV(pso);
      if (lane < 7) sts64(pc0 + (105 + lane) * 8, (v2u){0, 0});
      v4f aK[8], aV[8], aT[4];
#pragma unroll
      for (int m = 0; m < 8; ++m) { aK[m] = (v4f){1e-6f, 1e-6f, 1e-6f, 1e-6f}; aV[m] = (v4f){0, 0, 0, 0}; }
#pragma unroll
      for (int m = 0; m < 4; ++m) { const float c = isk ? 1e-6f : 0.f; aT[m] = (v4f){c, c, c, c}; }
      const unsigned x0 = l15 * 128 + ((grp ^ (l15 >> 1)) << 4);
      gemm_kv(aK, aV, aT, x0 + hf * 8192, x0 + (1 - hf) * 8192, 16384 + trl + wv * 40, 16384 + trr + (lane & 1) * 10256 + (wv >> 1) * 80 + (lane & 2) * 20 + 32 STPASS);
      if (EPRIO) __builtin_amdgcn_s_setprio(EPRIO);
      v4u tf[4], kf[4], vf[4];
#pragma unroll
      for (int t = 0; t < 2; ++t) {
        const v4u a = cat(PHIK(aT[2 * t]), PHIK(aT[2 * t + 1])), c = cat(pk4(aT[2 * t]), pk4(aT[2 * t + 1]));
#pragma unroll
        for (int d = 0; d < 4; ++d) tf[t][d] = isk ? a[d] : c[d];
        sts128(L_KVSLOT + wv * 2048 + t * 1024 + lane * 16, tf[t]);
      }
      asm volatile("" ::: "memory");
      sts32(L_CNT + 32 + wv * 4, 1u);
      v4f dA = {0, 0, 0, 0}, dB = dA, dC = dA, dD = dA;
      auto kvstep = [&](int t) {
        v4u tb;
#pragma unroll
        for (int d = 0; d < 4; ++d) tb[d] = own ? tf[t][d] : 0x3f803f80u;
        dA = mfma(kf[t], vf[t], dA); dB = mfma(kf[t], tb, dB); dC = mfma(tf[t], vf[t], dC); dD = mfma(tf[t], tb, dD);
      };
#pragma unroll
      for (int t = 0; t < 3; ++t) { kf[t] = cat(PHIK(aK[2 * t]), PHIK(aK[2 * t + 1])); vf[t] = cat(pk4(aV[2 * t]), pk4(aV[2 * t + 1])); }
      kvstep(0);
      PINV(kf[2]); PINV(vf[2]);
      asm volatile("" ::: "memory");
      unsigned mark = lds32(L_CNT + 32 + (wv ^ 1) * 4);
      asm volatile("" ::: "memory");
      tf[2] = lds128(L_KVSLOT + (wv ^ 1) * 2048 + lane * 16); tf[3] = lds128(L_KVSLOT + (wv ^ 1) * 2048 + 1024 + lane * 16);
      asm volatile("" ::: "memory");
      kf[3] = cat(PHIK(aK[6]), PHIK(aK[7])); vf[3] = cat(pk4(aV[6]), pk4(aV[7]));
      kvstep(1);
      PINV(kf[3]); PINV(vf[3]);
      if (QEARLY == 2) sts32(L_CNT + 32 + wv * 4, 2u);
      if (__builtin_amdgcn_readfirstlane(mark) == 0) {
        lds_wait<0>(L_CNT + 32 + (wv ^ 1) * 4, 1);
        tf[2] = lds128(L_KVSLOT + (wv ^ 1) * 2048 + lane * 16); tf[3] = lds128(L_KVSLOT + (wv ^ 1) * 2048 + 1024 + lane * 16);
      }
      kvstep(2); kvstep(3);
      sts64(pc0 + lane * 8, pk4(dA)); sts64(pb, pk4(dB)); sts64(pcc, pk4(dC)); sts64(pd, pk4(dD));
      waitv<0>();
      ST128(lds128(pc0 + lane * 16), bws, pso, 0, 16);
      arrive_set<!PHINT>(L_CNT, 4, p.ws, FL_P + g * 16 + rt, tag, lane);
      __builtin_amdgcn_s_setprio(0);
      ST(2);
      auto load_gate = [&]() {
        const v4i rq = mk_rsrc(p.q, 2048 * 1280);
        dma(rq, vw0, t0 * 1280 + cq * 320 + 160, L_QTB + wv * 1024); dma(rq, vw0, (t0 + 32) * 1280 + cq * 320 + 160, L_QTB + 5120 + wv * 1024);
        if (wv < 2) dma(rq, vw4, (t0 + wv * 32) * 1280 + cq * 320 + 160, L_QTB + wv * 5120 + 4096);
      };
      lds_wait<1>(L_CNT, 4); lds_wait<1>(L_CNT + 12, 4);
      if (QPRE == 2) load_gate();
      load_wo(0, 0);
      if (NPRE >= 2) { lds_wait<4>(L_CNT + 28, 4); load_wo(1, L_WQTOP - 25600); }
      if (NPRE == 3) { lds_wait<2>(L_CNT + 60, 4); load_wo(2, L_WQTOP - 51200); }
      if (QPRE == 1) load_gate();
      v4u selA, selB;
      { const unsigned one = (grp == (l15 >> 2)) ? ((l15 & 1) ? 0x3f800000u : 0x00003f80u) : 0u, d0 = (l15 & 2) ? 0u : one, d1 = (l15 & 2) ? one : 0u;
        selA = (v4u){d0, d1, 0, 0}; selB = (v4u){0, 0, d0, d1}; PINV(selA); PINV(selB); }
      if (PDELAY) __builtin_amdgcn_s_sleep(PDELAY);
      ST(4);
      wait_flags(p.ws, rep + FL_P + g * 16, 16, tag, lane);
      waitv<0>();
      ST(6);
#pragma unroll 1
      for (int pass = 0; pass < 2; ++pass) {
        v4u r[16];
#pragma unroll
        for (int j = 0; j < 16; ++j) { r[j] = __builtin_amdgcn_raw_buffer_load_b128(bws, po, ((j + 4 * bi) & 15) * PART_STRIDE, 16); SCHED(); }
        if (PHINT && pass == 0) arrive_hint(L_CNT + 4, 4, p.ws, FL_P2 + g * 16 + rt, tag, lane);
        v4f s0 = {0, 0, 0, 0}, s1 = s0;
        if (WFIRST && pass == 0) {
          lds_wait<1>(WFIRST == 1 ? L_CNT + 60 : L_CNT + 48, 4);
          load_wo(2, L_WQTOP - 51200);
          if (WFIRST >= 2) load_wo(3, L_WQTOP - 76800);
          if (WFIRST >= 3) load_gate();
        }
        if (SUMGATE) lds_wait<2>(L_CNT + 32 + wv * 4, 3);
#pragma unroll
        for (int j = 0; j < 16; ++j) { s0 = mfma(selA, r[j], s0); s1 = mfma(selB, r[j], s1); }
        sts128(L_KVC + tid * 16, cat(pk4(s0), pk4(s1)));
        if (!PHINT) break;
        const float chk = s0[0];
        if (!__any(chk != chk)) break;
        wait_flags(p.ws, rep + FL_P2 + g * 16, 16, tag, lane);
      }
      lds_arrive(L_CNT + 8, lane);
      ST(17);
      lds_wait<0>(L_CNT + 48, 4);
      if (NPRE == 1) load_wo(1, L_WQTOP - 25600);
      if (NPRE <= 2 && WFIRST < 1) load_wo(2, L_WQTOP - 51200);
      if (!WLATE && WFIRST < 2) load_wo(3, L_WQTOP - 76800);
      if (!QPRE && WFIRST < 3) load_gate();
      waitv<0>(); lds_arrive(L_CNT + 24, lane);
      lds_wait<2>(L_CNT + 24, 8);
      if (WLATE) load_wo(3, L_WQTOP - 76800);
    } else {
      auto piece = [&](int q) {
        const int st = q / 9, j = q % 9; const int ks = k0 + st >= 10 ? k0 + st - 10 : k0 + st; const unsigned so = ((st & 3) ^ 1) * L_KVSLOT;
        if (j < 4) dma(rx, (j & 1) ? xo1 : xo0, ks * 128 + (j >> 1) * 16 * 1280, so + lw * 4096 + j * 1024);
        else dma(rw, wo_[j - 4], kwcol + ks * 64 * 3840, so + kwl + (j - 4) * 1024);
      };
      auto wq_block = [&](int j, int i0, int i1) {
        int kb = 2 * k0 + j; kb = kb >= 20 ? kb - 20 : kb;
#pragma unroll
        for (int i = i0; i < i1; ++i) dma(rw, wo_[i], kb * 32 * 3840 + 160 * g, L_WQTOP - (j + 1) * 5120 + i * 1024);
      };
      GET_TAG();
      v4u xr[20][2];
      unsigned xa = (lw * 32 + l15) * 128 + ((grp ^ (l15 >> 1)) << 4), xb = xa + 2 * L_KVSLOT, xc = xa ^ 64, xd = xb ^ 64; PINV(xa); PINV(xb); PINV(xc); PINV(xd);
#define XRD(s) do { _Pragma("unroll") for (int hs = 0; hs < 2; ++hs) _Pragma("unroll") for (int m = 0; m < 2; ++m) \
        xr[2 * (s) + hs][m] = lds128((((s) & 2) ? (hs ? xd : xb) : (hs ? xc : xa)) + (((s) & 1) ^ 1) * L_KVSLOT + m * 2048); SCHED(); } while (0)
#define KVSTEP(s, q0, q1) do { _Pragma("unroll") for (int q = (q0); q < (q1); ++q) { piece(q); if ((s) > 0 && q == (q0) + XPOS) XRD((s) > 0 ? (s) - 1 : 0); } waitv<(q1) - 9 * ((s) + 1)>(); bar(); } while (0)
      KVSTEP(0, 9, 13); ST(14);
      KVSTEP(1, 13, 23); KVSTEP(2, 23, 33); KVSTEP(3, 33, 43); KVSTEP(4, 43, 53);
      KVSTEP(5, 53, 63); KVSTEP(6, 63, 73); KVSTEP(7, 73, 83); KVSTEP(8, 83, 90);
#undef KVSTEP
      ST(15);
      wq_block(lw, 0, 5); XRD(8); wq_block(4 + lw, 0, 2);
      waitv<7>(); bar();
      wq_block(4 + lw, 2, 5);
      XRD(9);
#undef XRD
      ST(13);
      wq_block(8 + lw, 0, 5); wq_block(12 + lw, 0, 5); wq_block(16 + lw, 0, 5);
      if (WSPLIT) { warm(ro, lane < 50 ? (bi * 200 + lw * 50 + lane) * 128 : OOB, 0); waitv<1>(); } else waitv<QEARLY == 1 ? 10 : 0>();
      lds_arrive(L_CNT + 12, lane);
      v4f acc[2][5];
#pragma unroll
      for (int m = 0; m < 2; ++m)
#pragma unroll
        for (int n = 0; n < 5; ++n) acc[m][n] = (v4f){1e-6f, 1e-6f, 1e-6f, 1e-6f};
      unsigned wa0 = L_WQTOP - 51200 + trl, wt0 = L_WQTOP - 51200 + trr + (lane & 3) * 40 + 32, wa1 = wa0 - 51200, wt1 = wt0 - 51200;
      PINV(wa0); PINV(wt0); PINV(wa1); PINV(wt1);
      lds_wait<1>(L_CNT + 12, 4);
      ST(1);
      v4u aC, aB, aZ, aX;
      v4u wf[2][5];
#define QRD(j) do { const unsigned o_ = (9 - (j) % 10) * 5120; _Pragma("unroll") for (int n = 0; n < 5; ++n) { \
        const unsigned a_ = n < 4 ? ((j) < 10 ? wa0 : wa1) + n * 40 + o_ : ((j) < 10 ? wt0 : wt1) + o_; wf[(j) & 1][n] = cat(ldtr(a_), ldtr(a_ + 2560)); } } while (0)
      QRD(0);
      if (QEARLY == 0) lds_wait<1>(L_CNT, 4);
      if (QEARLY == 2) lds_wait<1>(L_CNT + 32 + lw * 4, 2);
      SCHED();
      ST(2);
#pragma unroll
      for (int j = 0; j < 20; ++j) {
        if (QPRIO && j == 2) __builtin_amdgcn_s_setprio(QPRIO);
        if (j < 19) QRD(j + 1);
        if (j == 19) { aC = lds128(L_TAB + lane * 16); aB = lds128(L_TAB + 1024 + lane * 16); aZ = lds128(L_TAB + 2048 + lane * 16); aX = lds128(L_TAB + 3072 + lane * 16); }
#pragma unroll
        for (int n = 0; n < 5; ++n)
#pragma unroll
          for (int m = 0; m < 2; ++m) acc[m][n] = mfma(wf[j & 1][n], xr[j][m], acc[m][n]);
#pragma unroll
        for (int i = 0; i < 10; ++i) { __builtin_amdgcn_sched_group_barrier(0x8, 1, 0); __builtin_amdgcn_sched_group_barrier(0x100, 1, 0); }
        SCHED();
        if (QEARLY == 1 && j == 2) { waitv<0>(); lds_arrive(L_CNT + 68, lane); }
        if (QEARLY == 1 && j == 4) lds_wait<0>(L_CNT + 68, 4);
        if (j == 3 || j == 8 || j == 13) lds_arrive(j == 3 ? L_CNT + 28 : j == 8 ? L_CNT + 60 : L_CNT + 48, lane);
      }
#undef QRD
      if (SUMGATE) sts32(L_CNT + 32 + lw * 4, 3u);
      ST(3);
      v2u pq[2][5];
#pragma unroll
      for (int m = 0; m < 2; ++m)
#pragma unroll
        for (int n = 0; n < 5; ++n) pq[m][n] = PHIQ(acc[m][n]);
      v4u sq[2][4], sp[2][2];
#pragma unroll
      for (int m = 0; m < 2; ++m)
#pragma unroll
        for (int h = 0; h < 4; ++h) { sq[m][h] = cat(pq[m][h], pq[m][4]); PINV(sq[m][h]); if (h < 2) { sp[m][h] = cat(pq[m][2 * h], pq[m][2 * h + 1]); PINV(sp[m][h]); } }
      const unsigned ob = WS_OF + ((rt * 8 + 2 * lw) * 8 + g) * 2560;
      unsigned aA[4], oa = ob + lane * 16, oa1 = oa + 20480, ot = ob + aX.z; PINV(oa); PINV(oa1); PINV(ot);
#pragma unroll
      for (int h = 0; h < 4; ++h) { aA[h] = L_KVC + h * 896 + lane * 8; PINV(aA[h]); }
      waitv<0>();
      ST(4);
      __builtin_amdgcn_s_setprio(3);
      lds_wait<0>(L_CNT + 8, 4);
      ST(7);
      {
        const v2u z2 = {0, 0};
        v4u fo[4];
        const v4u fn0 = cat(lds64(aZ[0]), lds64(aZ[1])), fn1 = cat(lds64(aZ[2]), lds64(aZ[3])), fn2 = cat(z2, lds64(aX.y));
#pragma unroll
        for (int h = 0; h < 4; ++h) fo[h] = cat(lds64(aA[h]), lds64(aC[h]));
        const v4u ft0 = cat(lds64(aB[0]), lds64(aB[1])), ft1 = cat(lds64(aB[2]), lds64(aB[3])), ft2 = cat(z2, lds64(aX.x));
        SCHED();
        const v4f zero = {0, 0, 0, 0}, eps = {1e-6f, 1e-6f, 1e-6f, 1e-6f};
        v4f den[2], no[2][4], nt[2], rc[2];
        den[0] = mfma(fn0, sp[0][0], eps); den[1] = mfma(fn0, sp[1][0], eps); SCHED();
        den[0] = mfma(fn1, sp[0][1], den[0]); den[1] = mfma(fn1, sp[1][1], den[1]); SCHED();
        den[0] = mfma(fn2, sq[0][0], den[0]); den[1] = mfma(fn2, sq[1][0], den[1]); SCHED();
        no[0][0] = mfma(fo[0], sq[0][0], zero); no[0][1] = mfma(fo[1], sq[0][1], zero); SCHED();
#pragma unroll
        for (int i = 0; i < 6; ++i) {
          const int m = i >= 2, h = (i + 2) & 3;
          no[m][h] = mfma(fo[h], sq[m][h], zero);
          if (i < 4) { rc[i >> 1][2 * (i & 1)] = __builtin_amdgcn_rcpf(den[i >> 1][2 * (i & 1)]); rc[i >> 1][2 * (i & 1) + 1] = __builtin_amdgcn_rcpf(den[i >> 1][2 * (i & 1) + 1]); }
          SCHED();
        }
#pragma unroll
        for (int i = 0; i < 4; ++i) {
          const int m = i >> 1, h = 2 * (i & 1);
          if (i < 3) { nt[0] = mfma(i == 0 ? ft0 : i == 1 ? ft1 : ft2, i < 2 ? sp[0][i & 1] : sq[0][0], i ? nt[0] : zero); nt[1] = mfma(i == 0 ? ft0 : i == 1 ? ft1 : ft2, i < 2 ? sp[1][i & 1] : sq[1][0], i ? nt[1] : zero); }
          ST128(cat(pk4(no[m][h] * rc[m][h]), pk4(no[m][h + 1] * rc[m][h + 1])), bws, m ? oa1 : oa, (i & 1) * 1024, 16);
          SCHED();
        }
#pragma unroll
        for (int m = 0; m < 2; ++m) { const float ra = (lane & 16) ? rc[m][1] : rc[m][0], rb = (lane & 16) ? rc[m][3] : rc[m][2]; nt[m] *= (lane & 32) ? rb : ra; }
        ST128(conv_pack(nt[0], nt[1]), bws, ot, 0, 16);
        lds_arrive(L_CNT + 24, lane);
        ST(8);
        waitv<0>();
        set_flag(p.ws, FL_O + (rt * 4 + lw) * 8 + g, tag, 1, lane);
        lds_arrive(L_CNT + 16, lane);
      }
      __builtin_amdgcn_s_setprio(0);
      ST(9);
      out_offsets();
      lds_wait<1>(L_CNT + 24, 8);
      gate(L_QTB, L_YTB, 1);
      if (b == 0) {
        wait_flags(p.ws, FL_U + lw * 64, 64, tag, lane);
        lds_arrive(L_CNT + 56, lane);
        if (lw == 0) { lds_wait<1>(L_CNT + 56, 4); if (lane == 0) __hip_atomic_store((GAS u64*)p.ws, tag, RLX_AGENT); }
      }
      ST(11);
    }
  } else {
    if (wv < 4) {
      GET_TAG();
      out_offsets();
      const ConvArgs ca = conv_args(L_CONV + wv * L_CH, dbits, WS_YT + c0 * 4096, lane);
      waitv<0>(); bar();
      ST(1);
      {
        const int tg = lane & 7;
        unsigned ka = L_KIMG + tg * 2576 + (8 * wv + (lane >> 3)) * 4, sa = L_SW + (8 * wv + (lane >> 3)) * 16;
        unsigned uo = WS_UT + (160 * cq + 16 * wv + 2 * (lane >> 3)) * 4096 + 2 * (2040 - t0 - 8 * tg);
        unsigned kr[11], vr[8]; v4u sw;
#define PRD() do { PINV(ka); sw = lds128(sa); _Pragma("unroll") for (int j = 0; j < 11; ++j) kr[j] = lds32(ka + (j < 3 ? (5 + j) * 320 : 2576 + (j - 3) * 320)); \
          _Pragma("unroll") for (int j = 0; j < 8; ++j) vr[j] = lds32(ka + (L_VIMG - L_KIMG) + j * 320); ka += 128; sa += 512; } while (0)
        PRD();
        v4u ua[3], ub[3];
        ua[2] = ub[2] = (v4u){0, 0, 0, 0};
#pragma unroll
        for (int it = 0; it < 3; ++it) {
          if (it == 2 && wv >= 2) break;
          unsigned pa[10], pb[10]; v2f vv[8];
#pragma unroll
          for (int j = 0; j < 10; ++j) { pa[j] = __builtin_amdgcn_perm(kr[j], kr[j + 1], 0x05040100u); pb[j] = __builtin_amdgcn_perm(kr[j], kr[j + 1], 0x07060302u); }
#pragma unroll
          for (int j = 0; j < 8; ++j) vv[j] = (v2f){blo(vr[j]), bhi(vr[j])};
          const v4u w = sw;
          SCHED();
          if (it < 2) PRD();
          SCHED();
          v2f uu[8];
#pragma unroll
          for (int j = 0; j < 8; ++j) uu[j] = (v2f){dot0(pa[j], w.y), dot0(pb[j], w.w)};
          SCHED();
#pragma unroll
          for (int j = 0; j < 8; ++j) uu[j] = (v2f){dot2(pa[j + 2], w.x, uu[j].x), dot2(pb[j + 2], w.z, uu[j].y)};
          SCHED();
#pragma unroll
          for (int j = 0; j < 8; ++j) uu[j] *= vv[j];
          SCHED();
          ua[it] = (v4u){pk2(uu[7].x, uu[6].x), pk2(uu[5].x, uu[4].x), pk2(uu[3].x, uu[2].x), pk2(uu[1].x, uu[0].x)};
          ub[it] = (v4u){pk2(uu[7].y, uu[6].y), pk2(uu[5].y, uu[4].y), pk2(uu[3].y, uu[2].y), pk2(uu[1].y, uu[0].y)};
          PINV(ua[it]); PINV(ub[it]);
          SCHED();
          if (USTORE) { ST128(ua[it], bws, uo, it * 64 * 4096, 16); ST128(ub[it], bws, uo, (it * 64 + 1) * 4096, 16); SCHED(); }
        }
        ST(13);
        if (!USTORE) {
#pragma unroll
          for (int it = 0; it < 3; ++it) if (it < 2 || wv < 2) { ST128(ua[it], bws, uo, it * 64 * 4096, 16); ST128(ub[it], bws, uo, (it * 64 + 1) * 4096, 16); }
        }
        lds_arrive(L_CNT + 40, lane);
#undef PRD
      }
      ST(2);
      arrive_flag(L_CNT + 4, 4, p.ws, FL_U + cq * 32 + tt, tag, lane);
      ST(3);
      lds_wait<1>(L_CNT + 4, 4);
      if (UDELAY) __builtin_amdgcn_s_sleep(UDELAY);
      wait_flags(p.ws, rep + FL_U + (g >> 1) * 32 + wv * 8, 8, tag, lane);
      ST(6);
#pragma unroll
      for (int c = 4; c >= 0; --c) dma_sc1(rws, lane * 16, WS_UT + (c5 + c) * 4096 + (3 - wv) * 1024, L_CONV + c * L_CH + L_UOFF + (3 - wv) * 1024);
      waitv<4>();
      lds_arrive(L_CNT + 56, lane); lds_wait<0>(L_CNT + 56, 8);
      ST(4);
      conv_wave((4 - wv) * (int)L_CH + 128 * wv, ca, p.ws, wv, lane STPASS);
      ST(5);
      arrive_flag(L_CNT + 8, 4, p.ws, FL_Y + g * 16 + rt, tag, lane);
      ST(7);
      lds_wait<1>(L_CNT + 20, 4);
    } else {
      if (lw == 2) warm_x(2); else warm_w(2 + lw - (lw >> 1));
      GET_TAG();
      unsigned w1o = (lw & 1) ? off_w(5 + (lw >> 1)) : off_x(3 + (lw >> 1)), wqo;
      { const unsigned r5 = lane / 5, rho = 12 * lw + r5; int kb = k0 + (rho >> 2); kb = kb >= 10 ? kb - 10 : kb;
        wqo = (lane < 60 && rho < 40) ? (kb * 64 + iq * 4 + (rho & 3)) * 3840 + (xcd & 1) * 640 + (lane - r5 * 5) * 128 : OOB; PINV(wqo); }
      waitv<1>(); bar();
      warm((lw & 1) ? rw : rx, w1o, 0);
      const v4i rh = mk_rsrc(p.h, 640 * 4096);
#pragma unroll
      for (int c = 0; c < 5; ++c) dma(rh, lane * 16, (c5 + c) * 4096 + lw * 1024, L_CONV + c * L_CH + 32 + 3 * L_CS + lw * 1024);
#pragma unroll 1
      for (int c = lw; c < 5; c += 4) { const unsigned cb = L_CONV + c * L_CH;
        if (lane < 38) sts128(lane < 30 ? cb + L_UOFF + 4096 + lane * 16 : cb + ((lane - 30) >> 1) * L_CS + (lane & 1) * 16, (v4u){0, 0, 0, 0}); }
      waitv<0>();
      lds_arrive(L_CNT + 32, lane);
      lds_wait<2>(L_CNT + 40, 4);
      warm(rw, wqo, 0);
      row_offsets(); out_offsets();
      lds_wait<1>(L_CNT + 32, 4);
      ST(14);
#pragma unroll 1
      for (int c = 4; c >= 0; --c) {
        const unsigned a = L_CONV + c * L_CH + 32 + lw * 1024 + lane * 16;
        const v4u d = lds128(a + 3 * L_CS); const v2u m = lds64(a + 3 * L_CS - 8);
        const unsigned e0 = __builtin_amdgcn_alignbit(m.y, m.x, 16), e1 = __builtin_amdgcn_alignbit(d.x, m.y, 16), e2 = __builtin_amdgcn_alignbit(d.y, d.x, 16),
                       e3 = __builtin_amdgcn_alignbit(d.z, d.y, 16), e4 = __builtin_amdgcn_alignbit(d.w, d.z, 16);
        sts128(a, (v4u){e0, e1, e2, e3}); sts128(a + L_CS, (v4u){m.y, d.x, d.y, d.z}); sts128(a + 2 * L_CS, (v4u){e1, e2, e3, e4});
        if (c == 4) lds_arrive(L_CNT + 56, lane);
      }
      lds_arrive(L_CNT + 60, lane);
      unsigned yo5 = WS_YT + cd * 4096 + 16 * l15 + 256 * (grp >> 1), wov = lane < 50 ? (WSPLIT ? bi * 200 + lw * 50 + lane : iq * 400 + lw * 100 + lane) * 128 : OOB; PINV(yo5); PINV(wov);

      ST(13);
      lds_wait<2>(L_CNT + 56, 8);
      __builtin_amdgcn_s_setprio(3);
      warm(ro, wov, 0); if (!WSPLIT) warm(ro, wov, 6400);
      waitv<0>();
      v4u wr[25];
      {
        const __amdgpu_buffer_rsrc_t bo = __builtin_amdgcn_make_buffer_rsrc((void*)p.wo, 0, 640 * 1280, 0x00020000);
#pragma unroll
        for (int j = 0; j < 25; ++j) wr[j] = __builtin_amdgcn_raw_buffer_load_b128(bo, vo5[j % 5], ((lw ^ rot) * 5 + j / 5) * 32 * 1280 + gct * 160, 0);
      }
      SCHED();
      v4f ya, yb;
      {
        const float dv = blo(dbits);
        const unsigned ub = L_CONV5 + L_UOFF + 4088 - 32 * l15 - 8 * grp;
        const v2u ua = lds64(ub - 512 * lw), uc = lds64(ub - 512 * (7 - lw));
        ya = (v4f){mul1(dv, bhi(ua.y)), mul1(dv, blo(ua.y)), mul1(dv, bhi(ua.x)), mul1(dv, blo(ua.x))};
        yb = (v4f){mul1(dv, bhi(uc.y)), mul1(dv, blo(uc.y)), mul1(dv, bhi(uc.x)), mul1(dv, blo(uc.x))};
        PINV(ya); PINV(yb);
      }
      lds_wait<2>(L_CNT + 12, 4);
#pragma unroll
      for (int w = 0; w < 4; ++w) { ya = add4(ya, __builtin_bit_cast(v4f, lds128(L_RED + (lw * 4 + w) * 1024 + lane * 16))); yb = add4(yb, __builtin_bit_cast(v4f, lds128(L_RED + ((7 - lw) * 4 + w) * 1024 + lane * 16))); }
      conv_store(conv_pack(ya, yb), lw, yo5, p.ws, lane);
      lds_arrive(L_CNT + 12, lane); lds_wait<1>(L_CNT + 12, 8);
      ST(6);
      {
        const v4i rq = mk_rsrc(p.q, 2048 * 1280);
        if (lw < 2) {
#pragma unroll
          for (int j = 0; j < 5; ++j) dma(rq, vo5[j], (t0 + lw * 32) * 1280 + cq * 320, L_QT + lw * 5120 + j * 1024);
        } else if (!QPRE) warm(rq, (t0 + (lw - 2) * 32 + (lane >> 1)) * 1280 + cq * 320 + 160 + (lane & 1) * 156, 0);
      }
      waitv<0>();
      ST(17);
      bar();
      __builtin_amdgcn_s_setprio(0);
      unsigned wb = L_CONV + lw * 25600 + lane * 16; PINV(wb);
#pragma unroll
      for (int j = 0; j < 25; ++j) sts128(wb + j * 1024, wr[j]);
      lds_arrive(L_CNT + 20, lane);
      ST(7);
      gate(L_QT, L_YT, 0);
      ST(11);
    }
  }
  if (wv < 4) {
    ST(9);
    const int mt = (gr0 >> 4) + wv;
    unsigned wbq[4], wbt[4];
#pragma unroll
    for (int y = 0; y < 4; ++y) { const unsigned base = isA ? L_CONV + y * 25600 : y ? L_WQTOP - y * 25600 : 0;
      wbq[y] = base + trl; wbt[y] = base + (40 * grp + (l15 >> 2) + 16) * 160 + 8 * (lane & 3); PINV(wbq[y]); PINV(wbt[y]); }
    unsigned otl = (grp >> 1) * 2560 + 2048 + (grp & 1) * 256 + l15 * 16; PINV(otl);
    v4u wf[RING][5];
#define WRD(t, n) do { const int y_ = (t) / 5, s_ = (t) % 5; const unsigned ra = (s_ >> 1) * 80 + (s_ & 1) * 40; \
      wf[(t) % RING][n] = s_ < 4 ? cat(ldtr(wbq[y_] + ra * 160 + (n) * 32), ldtr(wbq[y_] + (ra + 20) * 160 + (n) * 32)) \
                              : cat(ldtr(wbt[y_] + (n) * 32), ldtr(wbt[y_] + 20 * 160 + (n) * 32)); } while (0)
#pragma unroll
    for (int f = 0; f < 5 * RING; ++f) WRD(f / 5, f % 5);
    SCHED();
    if (!isA && !BGATE) { lds_wait<1>(L_CNT + 16, 4); if (ODELAY) __builtin_amdgcn_s_sleep(ODELAY); }
    else { lds_wait<1>(L_CNT + 52, ACNT); if (ADELAY) __builtin_amdgcn_s_sleep(ADELAY); }
    wait_flags<OSLEEP>(p.ws, FL_O + (mt >> 1) * 8, 8, tag, lane);
    ST(10);
    v4u of[20];
#define ORD(t) do { const int s_ = (t) % 5; const unsigned oo = WS_OF + (mt * 8 + 2 * (((t) / 5) ^ rot)) * 2560; \
      if (s_ < 4) of[t] = __builtin_amdgcn_raw_buffer_load_b128(bws, lane * 16 + (s_ & 1) * 1024, oo + (s_ >> 1) * 2560, 16); \
      else of[t] = __builtin_amdgcn_raw_buffer_load_b128(bws, otl, oo, 16); } while (0)
#pragma unroll
    for (int t = 0; t < OAHEAD; ++t) { ORD(t); SCHED(); }
    SCHED();
    v4f acc[5];
#pragma unroll
    for (int n = 0; n < 5; ++n) acc[n] = (v4f){0, 0, 0, 0};
#pragma unroll
    for (int t = 0; t < 20; ++t) {
      v4u cur[5];
#pragma unroll
      for (int n = 0; n < 5; ++n) cur[n] = wf[t % RING][n];
#pragma unroll
      for (int f = 0; f < 100 - 5 * RING; ++f) if (t >= 1 && f >= (t - 1) * PACE / 4 && f < t * PACE / 4) WRD(RING + f / 5, f % 5);
#pragma unroll
      for (int n = 0; n < 5; ++n) acc[n] = mfma(cur[n], of[t], acc[n]);
      if (t + OAHEAD < 20) ORD(t + OAHEAD);
      __builtin_amdgcn_sched_group_barrier(0x8, 1, 0); __builtin_amdgcn_sched_group_barrier(0x100, 2, 0);
      __builtin_amdgcn_sched_group_barrier(0x8, 1, 0); __builtin_amdgcn_sched_group_barrier(0x100, 1, 0); __builtin_amdgcn_sched_group_barrier(0x20, 1, 0);
      __builtin_amdgcn_sched_group_barrier(0x8, 1, 0); __builtin_amdgcn_sched_group_barrier(0x100, 2, 0);
      __builtin_amdgcn_sched_group_barrier(0x8, 1, 0); __builtin_amdgcn_sched_group_barrier(0x100, 1, 0); __builtin_amdgcn_sched_group_barrier(0x20, 1, 0);
      __builtin_amdgcn_sched_group_barrier(0x8, 1, 0); __builtin_amdgcn_sched_group_barrier(0x100, 2, 0);
      SCHED();
    }
#undef WRD
#undef ORD
    v2u res[5];
#pragma unroll
    for (int n = 0; n < 5; ++n) res[n] = pk4(acc[n]);
    const unsigned stb = (isA ? L_STA : L_STB) + wv * 2560;
    OUT_TILE(res, stb, gr0 * 2560 + 1280 + gct * 160, AUX_B);
    ST(11);
  }
}

extern "C" void kernel_launch(void* const* d_in, const int* in_sizes, int n_in, void* d_out, int out_size, void* d_ws, size_t ws_size, hipStream_t stream) {
  Params p;
  p.q = (const u16*)d_in[0]; p.k = (const u16*)d_in[1]; p.v = (const u16*)d_in[2]; p.sk = (const u16*)d_in[3]; p.h = (const u16*)d_in[4];
  p.Dv = (const u16*)d_in[5]; p.x = (const u16*)d_in[6]; p.w = (const u16*)d_in[7]; p.wo = (const u16*)d_in[8];
  p.out = (u16*)d_out; p.ws = (char*)d_ws;
  fused_kernel<<<256, 512, 0, stream>>>(p);
}
```

```cpp
#include <hip/hip_runtime.h>
typedef unsigned short u16;
typedef unsigned long long u64;
typedef unsigned v2u __attribute__((ext_vector_type(2)));
typedef unsigned v4u __attribute__((ext_vector_type(4)));
typedef int v4i __attribute__((ext_vector_type(4)));
typedef float v2f __attribute__((ext_vector_type(2)));
typedef float v4f __attribute__((ext_vector_type(4)));
typedef short v4s __attribute__((ext_vector_type(4)));
typedef __bf16 v2bf __attribute__((ext_vector_type(2)));
typedef __bf16 v8bf __attribute__((ext_vector_type(8)));
#define GAS __attribute__((address_space(1)))
#define LAS __attribute__((address_space(3)))
#define RLX_AGENT __ATOMIC_RELAXED, __HIP_MEMORY_SCOPE_AGENT
#define DEVINL __device__ __forceinline__

constexpr unsigned WS_FLAG = 4096;
constexpr unsigned FL_U = 0, FL_P = 128, FL_Y = 256, FL_O = 384;
constexpr unsigned FL_N = 1024;
constexpr unsigned WS_UT = 2u << 20, WS_YT = 5u << 20, WS_PART = 8u << 20, WS_OF = 9u << 20;
constexpr unsigned WS_BYTES = 12u << 20;
constexpr unsigned PART_STRIDE = 3584;
constexpr unsigned OOB = 0x80000000u;
constexpr unsigned L_DUMMY = 162816, L_CNT = 163584;
constexpr unsigned L_KVSLOT = 36992, L_WQTOP = 4 * L_KVSLOT, L_PC = 148480, L_KVC = 152576, L_TAB = 156672, L_STB = 46080, L_QTB = 25600, L_YTB = 56320;
constexpr unsigned L_CS = 4160, L_UOFF = 4 * L_CS, L_CH = 21248;
constexpr unsigned L_KIMG = 0, L_VIMG = 9 * 2576, L_SW = 44032, L_RED = 0, L_QT = 0, L_YT = 20480, L_STA = 40960, L_CONV = 51200, L_CONV5 = L_CONV + 4 * L_CH;

struct Params { const u16 *q, *k, *v, *sk, *h, *Dv, *x, *w, *wo; u16* out; char* ws; };

DEVINL v4i mk_rsrc(const void* p, unsigned bytes) {
  u64 a = (u64)p; v4i r; r.x = (int)(unsigned)a; r.y = (int)((unsigned)(a >> 32) & 0xffffu); r.z = (int)bytes; r.w = 0x00020000; return r;
}
DEVINL void dma(v4i rs, unsigned voff, unsigned soff, unsigned lds) {
  asm volatile("s_mov_b32 m0, %3\n\ts_nop 0\n\tbuffer_load_dwordx4 %0, %1, %2 offen lds" :: "v"(voff), "s"(rs), "s"(soff), "s"(lds) : "memory");
}
DEVINL void dma_sc1(v4i rs, unsigned voff, unsigned soff, unsigned lds) {
  asm volatile("s_mov_b32 m0, %3\n\ts_nop 0\n\tbuffer_load_dwordx4 %0, %1, %2 offen sc1 lds" :: "v"(voff), "s"(rs), "s"(soff), "s"(lds) : "memory");
}
DEVINL void warm(v4i rs, unsigned voff, unsigned soff) {
  asm volatile("s_mov_b32 m0, %3\n\ts_nop 0\n\tbuffer_load_dword %0, %1, %2 offen lds" :: "v"(voff), "s"(rs), "s"(soff), "s"(L_DUMMY) : "memory");
}
template <int N> DEVINL void waitv() { asm volatile("s_waitcnt vmcnt(%0)" :: "n"(N) : "memory"); }
DEVINL void rawbar() { asm volatile("s_barrier" ::: "memory"); }
DEVINL void bar() { asm volatile("s_waitcnt lgkmcnt(0)\n\ts_barrier" ::: "memory"); }
#define SCHED() __builtin_amdgcn_sched_barrier(0)
DEVINL v4u lds128(unsigned a) { return *(const LAS v4u*)(size_t)a; }
DEVINL v2u lds64(unsigned a) { return *(const LAS v2u*)(size_t)a; }
DEVINL unsigned lds32(unsigned a) { return *(const LAS unsigned*)(size_t)a; }
DEVINL void sts128(unsigned a, v4u v) { *(LAS v4u*)(size_t)a = v; }
DEVINL void sts64(unsigned a, v2u v) { *(LAS v2u*)(size_t)a = v; }
DEVINL void sts32(unsigned a, unsigned v) { *(LAS unsigned*)(size_t)a = v; }
DEVINL v2u ldtr(unsigned a) { return __builtin_bit_cast(v2u, __builtin_amdgcn_ds_read_tr16_b64_v4i16((LAS v4s*)(size_t)a)); }
DEVINL v4u cat(v2u a, v2u b) { return (v4u){a.x, a.y, b.x, b.y}; }
DEVINL v4f mfma(v4u a, v4u b, v4f c) { return __builtin_amdgcn_mfma_f32_16x16x32_bf16(__builtin_bit_cast(v8bf, a), __builtin_bit_cast(v8bf, b), c, 0, 0, 0); }
DEVINL unsigned pk2(float a, float b) { v2f p = {a, b}; return __builtin_bit_cast(unsigned, __builtin_convertvector(p, v2bf)); }
DEVINL v2u pk4(v4f a) { return (v2u){pk2(a[0], a[1]), pk2(a[2], a[3])}; }
DEVINL float blo(unsigned u) { return __uint_as_float(u << 16); }
DEVINL float bhi(unsigned u) { return __uint_as_float(u & 0xffff0000u); }
DEVINL v2u phi4(v4f c) {
  const v4f a = c * 1.44269504089f - 1.44269504089e-6f;
  v4f r;
#pragma unroll
  for (int j = 0; j < 4; ++j) r[j] = __int_as_float(max(__float_as_int(c[j]), 0x358637bd));
  const v4f e = {__builtin_amdgcn_fmed3f(__builtin_amdgcn_exp2f(a[0]), 0.f, 1.f), __builtin_amdgcn_fmed3f(__builtin_amdgcn_exp2f(a[1]), 0.f, 1.f),
                 __builtin_amdgcn_fmed3f(__builtin_amdgcn_exp2f(a[2]), 0.f, 1.f), __builtin_amdgcn_fmed3f(__builtin_amdgcn_exp2f(a[3]), 0.f, 1.f)};
  return pk4(r + e);
}
DEVINL float add1(float a, float b) { float r; asm("v_add_f32 %0, %1, %2" : "=v"(r) : "v"(a), "v"(b)); return r; }
DEVINL float mul1(float a, float b) { float r; asm("v_mul_f32 %0, %1, %2" : "=v"(r) : "v"(a), "v"(b)); return r; }
DEVINL v4f add4(v4f a, v4f b) { return (v4f){add1(a[0], b[0]), add1(a[1], b[1]), add1(a[2], b[2]), add1(a[3], b[3])}; }
DEVINL v4f mul4(v4f a, v4f b) { return (v4f){mul1(a[0], b[0]), mul1(a[1], b[1]), mul1(a[2], b[2]), mul1(a[3], b[3])}; }
DEVINL float dot2(unsigned a, unsigned b, float c) { return __builtin_amdgcn_fdot2_f32_bf16(__builtin_bit_cast(v2bf, a), __builtin_bit_cast(v2bf, b), c, false); }
DEVINL float dot0(unsigned a, unsigned b) { float r; asm("v_dot2_f32_bf16 %0, %1, %2, 0" : "=v"(r) : "v"(a), "v"(b)); return r; }
#define ST128(v, rs, voff, soff, aux) __builtin_amdgcn_raw_buffer_store_b128(v, rs, (voff) + (soff), 0, aux)
#define PINV(x) asm volatile("" : "+v"(x))
#define PINS(x) asm volatile("" : "+s"(x))

DEVINL void set_flag(char* ws, unsigned idx, u64 tag, int nrep, int lane) {
  const __amdgpu_buffer_rsrc_t rs = __builtin_amdgcn_make_buffer_rsrc(ws, 0, WS_BYTES, 0x00020000);
  const unsigned lo = (unsigned)tag, hi = (unsigned)(tag >> 32);
  if (lane < 8 * nrep) ST128(((v4u){lo, hi, lo, hi}), rs, (lane >> 3) * (FL_N * 128) + (lane & 7) * 16, WS_FLAG + idx * 128, 16);
}
constexpr int NREP = 8;
#define KVDELAY 16
#define UDELAY 18
#define YDELAY 8
#define ODELAY 6
constexpr int RING = 9, PACE = 13, OAHEAD = 8, AUX_OUT = 2;
DEVINL void wait_flags(char* ws, unsigned idx, int n, u64 tag, int lane) {
  if (lane < n) {
    GAS u64* p = (GAS u64*)(ws + WS_FLAG + (idx + lane) * 128u);
#pragma unroll 1
    for (unsigned spins = 0; spins < (1u << 16); ++spins) {
      if (__hip_atomic_load(p, RLX_AGENT) == tag) break;
      __builtin_amdgcn_s_sleep(2);
    }
  }
  __builtin_amdgcn_fence(__ATOMIC_ACQUIRE, "wavefront");
  asm volatile("" ::: "memory");
}
DEVINL unsigned cnt_addr(unsigned cnt, int lane) { return lane ? L_DUMMY + 320 + lane * 4 : cnt; }
DEVINL void arrive_flag(unsigned cnt, unsigned n, char* ws, unsigned idx, u64 tag, int lane) {
  unsigned old;
  waitv<0>();
  asm volatile("ds_add_rtn_u32 %0, %1, %2\n\ts_waitcnt lgkmcnt(0)" : "=&v"(old) : "v"(cnt_addr(cnt, lane)), "v"(1u) : "memory");
  if (__builtin_amdgcn_readfirstlane(old) == n - 1) set_flag(ws, idx, tag, NREP, lane);
}
DEVINL void lds_arrive(unsigned cnt, int lane) {
  asm volatile("ds_add_u32 %0, %1" :: "v"(cnt_addr(cnt, lane)), "v"(1u) : "memory");
}
template <int SLEEP> DEVINL void lds_wait(unsigned cnt, unsigned n) {
  unsigned v, sv, spins;
  asm volatile("s_mov_b32 %2, 0x100000\n1:\n\tds_read_b32 %0, %3\n\ts_waitcnt lgkmcnt(0)\n\tv_readfirstlane_b32 %1, %0\n\ts_cmp_ge_u32 %1, %4\n\ts_cbranch_scc1 2f\n\t"
               "s_sleep %5\n\ts_sub_u32 %2, %2, 1\n\ts_cbranch_scc0 1b\n2:"
               : "=&v"(v), "=&s"(sv), "=&s"(spins) : "v"(cnt), "s"(n), "n"(SLEEP) : "memory", "scc");
}

DEVINL void gemm_kv(v4f (&aK)[8], v4f (&aV)[8], v4f (&aT)[4], unsigned xa, unsigned xb, unsigned wa, unsigned wt) {
  v4u xp[8], wp[3], xq[8], wq[3];
  unsigned so = L_KVSLOT;
#define RD(X, W, hs) do { X[0] = lds128((xa + so) ^ ((hs) * 64)); \
    W[0] = cat(ldtr(wa + so + (hs) * 5120), ldtr(wa + so + (hs) * 5120 + 2560)); \
    W[1] = cat(ldtr(wa + so + (hs) * 5120 + 10256), ldtr(wa + so + (hs) * 5120 + 2560 + 10256)); \
    W[2] = cat(ldtr(wt + so + (hs) * 5120), ldtr(wt + so + (hs) * 5120 + 2560)); \
    _Pragma("unroll") for (int m = 1; m < 8; ++m) X[m] = lds128((((m < 4 ? xa : xb) + so) ^ ((hs) * 64)) + (m & 3) * 2048); } while (0)
#define MM(X, W) do { _Pragma("unroll") for (int m = 0; m < 8; ++m) { aK[m] = mfma(X[m], W[0], aK[m]); aV[m] = mfma(X[m], W[1], aV[m]); if (m < 4) aT[m] = mfma(X[m], W[2], aT[m]); } } while (0)
#define MIX() do { _Pragma("unroll") for (int i = 0; i < 20; ++i) { __builtin_amdgcn_sched_group_barrier(0x8, 1, 0); __builtin_amdgcn_sched_group_barrier(0x100, 1, 0); } } while (0)
  bar(); SCHED();
  RD(xp, wp, 0);
  SCHED();
#pragma unroll 1
  for (int s = 0; s < 9; ++s) {
    RD(xq, wq, 1); MM(xp, wp); MIX(); SCHED();
    bar(); SCHED();
    so = (so == 0) ? 3 * L_KVSLOT : so - L_KVSLOT;
    RD(xp, wp, 0); MM(xq, wq); MIX(); SCHED();
  }
  RD(xq, wq, 1); MM(xp, wp); MIX(); SCHED();
#pragma unroll
  for (int m = 0; m < 4; ++m) aT[m] = mfma(xq[m], wq[2], aT[m]);
#pragma unroll
  for (int m = 0; m < 8; ++m) aK[m] = mfma(xq[m], wq[0], aK[m]);
#pragma unroll
  for (int m = 0; m < 8; ++m) aV[m] = mfma(xq[m], wq[1], aV[m]);
#undef RD
#undef MM
#undef MIX
}
struct ConvArgs { unsigned ha, ua, yo; v4u dg; };
DEVINL ConvArgs conv_args(unsigned cb, unsigned dbits, unsigned yoff, int lane) {
  const int i = lane & 15, g = lane >> 4, jt = 15 - i - 8 * g;
  ConvArgs c;
  c.ha = cb + (i >> 2) * L_CS + 16 * g + 8 * (i & 3) + 8;
  c.ua = cb + L_UOFF + 480 - 32 * i + 16 * g;
  c.yo = yoff + 16 * i + 256 * (g >> 1);
#pragma unroll
  for (int d = 0; d < 4; ++d) c.dg[d] = (jt == 2 * d) ? dbits : (jt == 2 * d + 1) ? (dbits << 16) : 0u;
  PINV(c.ha); PINV(c.ua); PINV(c.yo); PINV(c.dg);
  return c;
}
DEVINL v4u conv_pack(v4f a, v4f b) {
  v2u X = pk4(a), Y = pk4(b);
  auto r0 = __builtin_amdgcn_permlane16_swap(X.x, Y.x, false, false);
  auto r1 = __builtin_amdgcn_permlane16_swap(X.y, Y.y, false, false);
  return (v4u){r0[0], r1[0], r0[1], r1[1]};
}
DEVINL void conv_store(v4u o, int ta, unsigned yo, char* ws, int lane) {
  const __amdgpu_buffer_rsrc_t rs = __builtin_amdgcn_make_buffer_rsrc(ws, 0, WS_BYTES, 0x00020000);
  ST128(o, rs, yo + ((lane & 16) ? 7 - ta : ta) * 512, 0, 16);
}
DEVINL void conv_wave(int off5, ConvArgs c, char* ws, int wv, int lane) {
  unsigned ha = c.ha + off5, ua = c.ua + off5;
  v4f acc[8], ac5[8];
  v4u F[8], G[8], F2[8], G2[8];
#define CRD(F, G) do { F[0] = cat(ldtr(ha), ldtr(ha + 8)); _Pragma("unroll") for (int v = 0; v < 8; ++v) G[v] = lds128(ua + (7 - v) * 512); \
    _Pragma("unroll") for (int v = 1; v < 8; ++v) F[v] = cat(ldtr(ha + v * 512), ldtr(ha + v * 512 + 8)); ha += 64; ua += 64; PINV(ha); PINV(ua); } while (0)
#define CMM(A, F, G) do { _Pragma("unroll") for (int w = 0; w < 8; ++w) _Pragma("unroll") for (int t = w; t < 8; ++t) A[t] = mfma(F[w], G[t - w], A[t]); } while (0)
#define CMIX() do { _Pragma("unroll") for (int j = 0; j < 36; ++j) { __builtin_amdgcn_sched_group_barrier(0x8, 1, 0); __builtin_amdgcn_sched_group_barrier(0x100, 1, 0); } } while (0)
  CRD(F, G);
  waitv<0>(); lds_arrive(L_CNT + 60, lane);
  SCHED();
#pragma unroll
  for (int t = 0; t < 8; ++t) ac5[t] = (v4f){0, 0, 0, 0};
  CRD(F2, G2); CMM(ac5, F, G); CMIX(); SCHED();
  lds_wait<0>(L_CNT + 60, 8);
  ha = c.ha; ua = c.ua;
  CRD(F, G); CMM(ac5, F2, G2); CMIX(); SCHED();
  {
    unsigned ra = L_RED + wv * 1024 + lane * 16;
#pragma unroll
    for (int t = 0; t < 8; ++t) acc[t] = mfma(c.dg, G[t], (v4f){0, 0, 0, 0});
    CRD(F2, G2); CMM(acc, F, G);
#pragma unroll
    for (int t = 0; t < 8; ++t) sts128(ra + t * 4096, __builtin_bit_cast(v4u, ac5[t]));
#pragma unroll
    for (int j = 0; j < 24; ++j) { __builtin_amdgcn_sched_group_barrier(0x8, 1, 0); __builtin_amdgcn_sched_group_barrier(0x100, 1, 0); }
#pragma unroll
    for (int j = 0; j < 8; ++j) { __builtin_amdgcn_sched_group_barrier(0x8, 2, 0); __builtin_amdgcn_sched_group_barrier(0x200, 1, 0); }
    SCHED();
    lds_arrive(L_CNT + 12, lane);
  }
#pragma unroll 1
  for (int it = 0; it < 3; ++it) {
    CRD(F, G); CMM(acc, F2, G2); CMIX(); SCHED();
    CRD(F2, G2); CMM(acc, F, G); CMIX(); SCHED();
  }
  bar(); SCHED();
#undef CRD
#undef CMM
#undef CMIX
#pragma unroll
  for (int a = 0; a < 4; ++a) {
    const int b = 7 - a;
#pragma unroll
    for (int w = 0; w <= b; ++w) { acc[b] = mfma(F2[w], G2[b - w], acc[b]); if (w <= a) acc[a] = mfma(F2[w], G2[a - w], acc[a]); }
  }
#pragma unroll
  for (int a = 0; a < 4; ++a) conv_store(conv_pack(acc[a], acc[7 - a]), a, c.yo, ws, lane);
  __builtin_amdgcn_sched_group_barrier(0x8, 12, 0);
#pragma unroll
  for (int a = 0; a < 3; ++a) {
#pragma unroll
    for (int j = 0; j < 8; ++j) { __builtin_amdgcn_sched_group_barrier(0x2, 1, 0); __builtin_amdgcn_sched_group_barrier(0x8, 1, 0); }
    __builtin_amdgcn_sched_group_barrier(0x40, 1, 0);
  }
}

__global__ void __launch_bounds__(512, 2) fused_kernel(Params p) {
  __shared__ __attribute__((aligned(1024))) char smem[163840];
  u64 pc; unsigned code_bytes;
  asm volatile("s_getpc_b64 %0\n.Lfk_head:\n\ts_mov_b32 %1, .Lfk_tail-.Lfk_head" : "=s"(pc), "=s"(code_bytes));
  const int tid = threadIdx.x, lane = tid & 63;
  const int b = blockIdx.x, xcd = b & 7, bi = b >> 3;
  warm(mk_rsrc((const void*)pc, code_bytes), tid < 38 ? ((bi & 7) * 38 + tid) * 128 : OOB, 0);
  const int wv = __builtin_amdgcn_readfirstlane(tid >> 6);
  const bool isA = bi >= 16;
  const int rt = 4 * (xcd >> 1) + (bi & 3), g = 4 * (xcd & 1) + ((bi >> 2) & 3);
  typedef __attribute__((address_space(4))) const u64 cu64;
  typedef __attribute__((address_space(4))) const unsigned cu32;
  const int lw = wv & 3, iq = bi & 15;
  int k0 = ((xcd >> 1) * 5 >> 1) + 5 * (xcd & 1); k0 = k0 >= 10 ? k0 - 10 : k0;
  v4i rx, rw;
#define MK_XW() do { const u16 *x_ = p.x, *w_ = p.w; PINS(x_); PINS(w_); rx = mk_rsrc(x_, 2048 * 1280); rw = mk_rsrc(w_, 640 * 3840); } while (0)
  auto off_x = [&](int j) {
    int ka = k0 + 2 * j + (lane >> 5); ka = ka >= 10 ? ka - 10 : ka;
    unsigned vo = ((xcd >> 1) * 512 + iq * 32 + (lane & 31)) * 1280 + ka * 128; PINV(vo);
    return vo;
  };
  auto off_w = [&](int j) {
    const unsigned r10 = lane / 10, c10 = lane - r10 * 10, rho = 6 * j + r10; int kb = k0 + (rho >> 2); kb = kb >= 10 ? kb - 10 : kb;
    unsigned vo = (kb * 64 + iq * 4 + (rho & 3)) * 3840 + 1280 + (xcd & 1) * 640 + c10 * 128 + (c10 >= 5 ? 640 : 0); PINV(vo);
    vo = (lane < 60 && rho < 40) ? vo : OOB; PINV(vo);
    return vo;
  };
  auto warm_x = [&](int j) { warm(rx, off_x(j), 0); };
  auto warm_w = [&](int j) { warm(rw, off_w(j), 0); };
  unsigned xo0 = 0, xo1 = 0, wo_[5] = {0, 0, 0, 0, 0};
  auto loader_offsets = [&]() {
    const unsigned r = lane >> 3;
    xo0 = (rt * 128 + lw * 32 + r) * 1280 + (((lane & 7) ^ (r >> 1)) << 4);
    xo1 = (xo0 ^ 64) + 8 * 1280;
#pragma unroll
    for (int j = 0; j < 5; ++j) { const unsigned ch = j * 64 + lane, rho = ch / 10, c = ch - rho * 10;
      wo_[j] = (8 * ((rho >> 2) & 3) + 4 * (rho >> 4) + (rho & 3)) * 3840 + c * 16; PINV(wo_[j]); }
    PINV(xo0); PINV(xo1);
  };
  const unsigned kwcol = (640 + 640 * (lw & 1) + 80 * g) * 2 + (lw >> 1) * 32 * 3840, kwl = 16384 + (lw & 1) * 10256 + (lw >> 1) * 5120;
  auto issue_x = [&](int ks, unsigned so) {
#pragma unroll
    for (int j = 0; j < 4; ++j) dma(rx, (j & 1) ? xo1 : xo0, ks * 128 + (j >> 1) * 16 * 1280, so + lw * 4096 + j * 1024);
  };
  auto issue_kw = [&](int ks, unsigned so) {
#pragma unroll
    for (int j = 0; j < 5; ++j) dma(rw, wo_[j], kwcol + ks * 64 * 3840, so + kwl + j * 1024);
  };
  const int tt = 4 * xcd + (iq >> 2), cq = iq & 3, t0 = tt * 64;
  if (isA) {
    const unsigned r20 = lane / 20, so = (t0 + wv * 8) * 1280, la = L_KIMG + 2576 + wv * 2576;
    unsigned vo = r20 * 1280 + (lane - r20 * 20) * 16 + cq * 320; PINV(vo);
    const bool wx = wv == 0 || wv == 3; const unsigned xv = off_x(wv & 1), wz = off_w(wv - 1);
    MK_XW();
    warm(wx ? rx : rw, wv > 3 ? OOB : wx ? xv : wz, 0);
    __builtin_amdgcn_s_sleep(KVDELAY);
    const v4i rk = mk_rsrc(p.k, 2048 * 1280), rv = mk_rsrc(p.v, 2048 * 1280);
    if (lane < 60) {
      dma(rk, vo, so, la); dma(rk, vo, so + 3840, la + 960); dma(rv, vo, so, la + 8 * 2576); dma(rv, vo, so + 3840, la + 8 * 2576 + 960);
      if (wv == 7) dma(rk, tt ? vo + (t0 - 3) * 1280 : OOB, 0, L_KIMG + 1600);
      if (lane < 40) {
        dma(rk, vo, so + 7680, la + 1920); dma(rv, vo, so + 7680, la + 8 * 2576 + 1920);
        if (wv == 5 || wv == 6) dma(mk_rsrc(p.sk, 640 * 8), cq * 1280 + (wv - 5) * 640 + lane * 16, 0, L_SW + (wv - 5) * 640);
      }
    }
  } else if (wv >= 4) { loader_offsets(); MK_XW(); issue_x(k0, L_KVSLOT); issue_kw(k0, L_KVSLOT); }
  else MK_XW();
  asm volatile("" ::: "memory");
  const int c5 = 80 * g + 5 * rt, c0 = c5 + lw, cd = wv < 4 ? c0 : c5 + 4;
  const v4i rws = mk_rsrc(p.ws, WS_BYTES), ro = mk_rsrc(p.wo, 640 * 1280);
  const __amdgpu_buffer_rsrc_t bws = __builtin_amdgcn_make_buffer_rsrc(p.ws, 0, WS_BYTES, 0x00020000);
  const __amdgpu_buffer_rsrc_t bout = __builtin_amdgcn_make_buffer_rsrc(p.out, 0, 2048 * 2560, 0x00020000);
  const unsigned rep = (xcd & (NREP - 1)) * FL_N;
  const int l15 = lane & 15, grp = lane >> 4;
  const unsigned trr = (4 * grp + (l15 >> 2)) * 160, trl = trr + 8 * (lane & 3);
  const int gr0 = (4 * xcd + ((bi >> 2) & 3)) * 64, gct = (bi & 3) + 4 * (bi >> 4), rot = bi & 3;
  unsigned vo5[5];
  auto row_offsets = [&]() {
#pragma unroll
    for (int j = 0; j < 5; ++j) { const unsigned ch = j * 64 + lane, r = ch / 10, c = ch - r * 10; vo5[j] = r * 1280 + c * 16; PINV(vo5[j]); }
  };
  unsigned oo3[3];
  auto out_offsets = [&]() {
#pragma unroll
    for (int j = 0; j < 3; ++j) { const unsigned ch = j * 64 + lane, r = ch / 10; oo3[j] = ch < 160 ? (lw * 16 + r) * 2560 + (ch - r * 10) * 16 : OOB; PINV(oo3[j]); }
  };
#define OUT_TILE(res, sta, soff, aux) do { const unsigned w_ = (sta) + l15 * 160 + grp * 8; \
    _Pragma("unroll") for (int n = 0; n < 5; ++n) sts64(w_ + n * 32, res[n]); \
    asm volatile("" ::: "memory"); \
    _Pragma("unroll") for (int j = 0; j < 3; ++j) ST128(lds128((sta) + j * 1024 + lane * 16), bout, oo3[j], soff, aux); } while (0)
  u64 tag; unsigned dbits;
#define GET_TAG() do { const char* w_ = p.ws; const u16* d_ = p.Dv; PINS(w_); PINS(d_); u64 e_ = *(cu64*)w_; dbits = ((cu32*)d_)[cd >> 1]; PINS(e_); PINS(dbits); \
    dbits = (dbits >> (16 * (cd & 1))) & 0xffffu; tag = e_ * 6364136223846793005ull + 1442695040888963407ull; PINS(tag); } while (0)
  auto gate = [&](unsigned lq, unsigned ly, int tl) {
    const unsigned sta = lq + lw * 2560;
    v4f qf[5];
#pragma unroll
    for (int n = 0; n < 5; ++n) { const v2u u = lds64(sta + l15 * 160 + grp * 8 + n * 32); qf[n] = (v4f){blo(u.x), bhi(u.x), blo(u.y), bhi(u.y)}; PINV(qf[n]); }
    unsigned yo = (lane >> 3) * 4096 + (lane & 3) * 16 + (lane & 4) * 64; PINV(yo);
    unsigned yl = ly + (4 * grp + (l15 >> 2)) * 128 + (lane & 2) * 32 + lw * 16 + (lane & 1) * 8; PINV(yl);
    const unsigned ys = WS_YT + (160 * cq + 80 * tl + 20 * lw) * 4096 + (t0 >> 8) * 512 + (t0 & 192);
    if (tl == 0) { lds_wait<2>(L_CNT + 8, 4); __builtin_amdgcn_s_sleep(YDELAY); }
    wait_flags(p.ws, rep + FL_Y + cq * 32 + tl * 16 + lw * 4, 4, tag, lane);
    dma_sc1(rws, yo, ys, ly + lw * 2560); dma_sc1(rws, yo, ys + 8 * 4096, ly + lw * 2560 + 1024);
    if (lane < 32) dma_sc1(rws, yo, ys + 16 * 4096, ly + lw * 2560 + 2048);
    lds_arrive(L_CNT + 52, lane);
    waitv<0>();
    lds_arrive(L_CNT + 64, lane); lds_wait<0>(L_CNT + 64, 4);
    v2u res[5];
#pragma unroll
    for (int n = 0; n < 5; ++n) { const v2u u = ldtr(yl + n * 2048); res[n] = pk4(mul4(qf[n], (v4f){blo(u.x), bhi(u.x), blo(u.y), bhi(u.y)})); }
    OUT_TILE(res, sta, t0 * 2560 + cq * 320 + tl * 160, AUX_OUT);
  };
  if (tid == 0) { asm volatile("" :: "v"(smem)); sts64(L_CNT + 64, (v2u){0, 0}); sts128(L_CNT, (v4u){0, 0, 0, 0}); sts128(L_CNT + 16, (v4u){0, 0, 0, 0}); sts128(L_CNT + 32, (v4u){0, 0, 0, 0}); sts128(L_CNT + 48, (v4u){0, 0, 0, 0}); }

  if (!isA) {
    unsigned vw0, vw4;
    auto wo_offsets = [&]() { unsigned l_ = lane; PINV(l_); const unsigned c0_ = lw * 64 + l_, r0 = c0_ / 10, c4 = 256 + l_, r4 = c4 / 10;
      vw0 = r0 * 1280 + (c0_ - r0 * 10) * 16; vw4 = r4 * 1280 + (c4 - r4 * 10) * 16; PINV(vw0); PINV(vw4); };
    auto load_wo = [&](int y, unsigned base) {
      int gb = gct; PINS(gb);
      dma(ro, vw4, ((y ^ rot) * 5 + lw) * 32 * 1280 + gb * 160, base + lw * 5120 + 4096);
      if (lw == y) dma(ro, vw4, ((y ^ rot) * 5 + 4) * 32 * 1280 + gb * 160, base + 4 * 5120 + 4096);
#pragma unroll
      for (int i = 0; i < 5; ++i) dma(ro, vw0, ((y ^ rot) * 5 + i) * 32 * 1280 + gb * 160, base + i * 5120 + lw * 1024);
    };
    if (wv < 4) {
      GET_TAG();
      const int hf = wv & 1;
      const bool isk = !(l15 & 4), own = (l15 >> 2) == 2 * hf + 1;
      const unsigned pc0 = L_PC + wv * 896, trash = L_DUMMY + 256;
      const int jb = own ? (l15 & 3) : l15 == 0 ? 4 : -1;
      unsigned pb = jb >= 0 ? pc0 + (80 + 5 * grp + jb) * 8 : trash, pcc = grp == 2 * hf ? pc0 + (64 + l15) * 8 : trash,
               pd = (grp == 2 * hf && jb >= 0) ? pc0 + (100 + jb) * 8 : trash;
      PINV(pb); PINV(pcc); PINV(pd);
      out_offsets(); wo_offsets();
      {
        const unsigned c_ = l15 >> 2, r_ = l15 & 3, zu = L_KVC + 3584;
        const unsigned sel = wv == 0 ? grp : wv == 1 ? c_ : r_;
        const unsigned a = L_KVC + sel * 896 + (wv == 0 ? 64 + l15 : wv == 1 ? 80 + 5 * grp + r_ : 84 + 5 * grp) * 8;
        v4u t = {sel == 0 ? a : zu, sel == 1 ? a : zu, sel == 2 ? a : zu, sel == 3 ? a : zu};
        if (wv == 3) t = (v4u){(unsigned)grp == c_ ? L_KVC + c_ * 896 + (100 + r_) * 8 : zu, (unsigned)grp == r_ ? L_KVC + r_ * 896 + 104 * 8 : zu,
                               (unsigned)((grp & 1) * 20480 + 2048 + (grp >> 1) * 256 + l15 * 16), 0};
        sts128(L_TAB + wv * 1024 + lane * 16, t);
      }
      unsigned po = (tid < 224) ? WS_PART + g * 16 * PART_STRIDE + tid * 16 : OOB, pso = lane < 56 ? WS_PART + (g * 16 + rt) * PART_STRIDE + wv * 896 + lane * 16 : OOB;
      PINV(po); PINV(pso);
      if (lane < 7) sts64(pc0 + (105 + lane) * 8, (v2u){0, 0});
      v4f aK[8], aV[8], aT[4];
#pragma unroll
      for (int m = 0; m < 8; ++m) { aK[m] = (v4f){1e-6f, 1e-6f, 1e-6f, 1e-6f}; aV[m] = (v4f){0, 0, 0, 0}; }
#pragma unroll
      for (int m = 0; m < 4; ++m) { const float c = isk ? 1e-6f : 0.f; aT[m] = (v4f){c, c, c, c}; }
      const unsigned x0 = l15 * 128 + ((grp ^ (l15 >> 1)) << 4);
      gemm_kv(aK, aV, aT, x0 + hf * 8192, x0 + (1 - hf) * 8192, 16384 + trl + wv * 40, 16384 + trr + (lane & 1) * 10256 + (wv >> 1) * 80 + (lane & 2) * 20 + 32);
      __builtin_amdgcn_s_setprio(2);
      v4u tf[4], kf[4], vf[4];
#pragma unroll
      for (int t = 0; t < 2; ++t) {
        const v4u a = cat(phi4(aT[2 * t]), phi4(aT[2 * t + 1])), c = cat(pk4(aT[2 * t]), pk4(aT[2 * t + 1]));
#pragma unroll
        for (int d = 0; d < 4; ++d) tf[t][d] = isk ? a[d] : c[d];
        sts128(L_KVSLOT + wv * 2048 + t * 1024 + lane * 16, tf[t]);
      }
      asm volatile("" ::: "memory");
      sts32(L_CNT + 32 + wv * 4, 1u);
      v4f dA = {0, 0, 0, 0}, dB = dA, dC = dA, dD = dA;
      auto kvstep = [&](int t) {
        v4u tb;
#pragma unroll
        for (int d = 0; d < 4; ++d) tb[d] = own ? tf[t][d] : 0x3f803f80u;
        dA = mfma(kf[t], vf[t], dA); dB = mfma(kf[t], tb, dB); dC = mfma(tf[t], vf[t], dC); dD = mfma(tf[t], tb, dD);
      };
#pragma unroll
      for (int t = 0; t < 4; ++t) { kf[t] = cat(phi4(aK[2 * t]), phi4(aK[2 * t + 1])); PINV(kf[t]); }
      asm volatile("" ::: "memory");
      sts32(L_CNT + 32 + wv * 4, 2u);
      unsigned mark = lds32(L_CNT + 32 + (wv ^ 1) * 4);
      asm volatile("" ::: "memory");
      tf[2] = lds128(L_KVSLOT + (wv ^ 1) * 2048 + lane * 16); tf[3] = lds128(L_KVSLOT + (wv ^ 1) * 2048 + 1024 + lane * 16);
      SCHED();
#pragma unroll
      for (int t = 0; t < 4; ++t) vf[t] = cat(pk4(aV[2 * t]), pk4(aV[2 * t + 1]));
      kvstep(0); kvstep(1);
      PINV(vf[2]); PINV(vf[3]);
      if (__builtin_amdgcn_readfirstlane(mark) == 0) {
        lds_wait<0>(L_CNT + 32 + (wv ^ 1) * 4, 1);
        tf[2] = lds128(L_KVSLOT + (wv ^ 1) * 2048 + lane * 16); tf[3] = lds128(L_KVSLOT + (wv ^ 1) * 2048 + 1024 + lane * 16);
      }
      kvstep(2); kvstep(3);
      sts64(pc0 + lane * 8, pk4(dA)); sts64(pb, pk4(dB)); sts64(pcc, pk4(dC)); sts64(pd, pk4(dD));
      asm volatile("" ::: "memory");
      ST128(lds128(pc0 + lane * 16), bws, pso, 0, 16);
      arrive_flag(L_CNT, 4, p.ws, FL_P + g * 16 + rt, tag, lane);
      __builtin_amdgcn_s_setprio(0);
      auto load_gate = [&]() {
        const v4i rq = mk_rsrc(p.q, 2048 * 1280);
        dma(rq, vw0, t0 * 1280 + cq * 320 + 160, L_QTB + wv * 1024); dma(rq, vw0, (t0 + 32) * 1280 + cq * 320 + 160, L_QTB + 5120 + wv * 1024);
        if (wv < 2) dma(rq, vw4, (t0 + wv * 32) * 1280 + cq * 320 + 160, L_QTB + wv * 5120 + 4096);
      };
      lds_wait<1>(L_CNT, 4); lds_wait<1>(L_CNT + 12, 4);
      load_wo(0, 0);
      lds_wait<4>(L_CNT + 28, 4);
      load_wo(1, L_WQTOP - 25600);
      v4u selA, selB;
      { const unsigned one = (grp == (l15 >> 2)) ? ((l15 & 1) ? 0x3f800000u : 0x00003f80u) : 0u, d0 = (l15 & 2) ? 0u : one, d1 = (l15 & 2) ? one : 0u;
        selA = (v4u){d0, d1, 0, 0}; selB = (v4u){0, 0, d0, d1}; PINV(selA); PINV(selB); }
      wait_flags(p.ws, rep + FL_P + g * 16, 16, tag, lane);
      {
        v4u r[16];
#pragma unroll
        for (int j = 0; j < 16; ++j)
          asm volatile("buffer_load_dwordx4 %0, %1, %2, %3 offen sc1" : "=&v"(r[j]) : "v"(po), "s"(rws), "s"(((j + 4 * bi) & 15) * PART_STRIDE) : "memory");
        lds_wait<1>(L_CNT + 60, 4);
        load_wo(2, L_WQTOP - 51200);
        v4f s0 = {0, 0, 0, 0}, s1 = s0;
#define SUMSTEP(j) do { asm volatile("s_waitcnt vmcnt(%1)" : "+v"(r[j]) : "n"(15 - (j) + 6) : "memory"); s0 = mfma(selA, r[j], s0); s1 = mfma(selB, r[j], s1); SCHED(); } while (0)
        SUMSTEP(0); SUMSTEP(1); SUMSTEP(2); SUMSTEP(3); SUMSTEP(4); SUMSTEP(5); SUMSTEP(6); SUMSTEP(7);
        SUMSTEP(8); SUMSTEP(9); SUMSTEP(10); SUMSTEP(11); SUMSTEP(12); SUMSTEP(13); SUMSTEP(14); SUMSTEP(15);
#undef SUMSTEP
        sts128(L_KVC + tid * 16, cat(pk4(s0), pk4(s1)));
      }
      lds_arrive(L_CNT + 8, lane);
      lds_wait<0>(L_CNT + 48, 4);
      load_wo(3, L_WQTOP - 76800);
      load_gate();
      waitv<0>(); lds_arrive(L_CNT + 24, lane);
      lds_wait<2>(L_CNT + 24, 8);
    } else {
      auto wq_block = [&](int j, int i0, int i1) {
        int kb = 2 * k0 + j; kb = kb >= 20 ? kb - 20 : kb;
#pragma unroll
        for (int i = i0; i < i1; ++i) dma(rw, wo_[i], kb * 32 * 3840 + 160 * g, L_WQTOP - (j + 1) * 5120 + i * 1024);
      };
      GET_TAG();
      v4u xr[20][2];
      unsigned xa = (lw * 32 + l15) * 128 + ((grp ^ (l15 >> 1)) << 4), xb = xa + 2 * L_KVSLOT, xc = xa ^ 64, xd = xb ^ 64; PINV(xa); PINV(xb); PINV(xc); PINV(xd);
#define XRD(s) do { _Pragma("unroll") for (int hs = 0; hs < 2; ++hs) _Pragma("unroll") for (int m = 0; m < 2; ++m) \
        xr[2 * (s) + hs][m] = lds128((((s) & 2) ? (hs ? xd : xb) : (hs ? xc : xa)) + (((s) & 1) ^ 1) * L_KVSLOT + m * 2048); SCHED(); } while (0)
#define KVSTEP(s) do { const int ks = k0 + (s) + 1 >= 10 ? k0 + (s) - 9 : k0 + (s) + 1; const unsigned so = ((((s) + 1) & 3) ^ 1) * L_KVSLOT; \
        issue_x(ks, so); if ((s) > 0) XRD((s) > 0 ? (s) - 1 : 0); issue_kw(ks, so); waitv<9>(); bar(); } while (0)
      KVSTEP(0);
      KVSTEP(1); KVSTEP(2); KVSTEP(3); KVSTEP(4); KVSTEP(5); KVSTEP(6); KVSTEP(7); KVSTEP(8);
#undef KVSTEP
      wq_block(lw, 0, 5); XRD(8); wq_block(4 + lw, 0, 2);
      waitv<7>(); bar();
      wq_block(4 + lw, 2, 5);
      XRD(9);
#undef XRD
      unsigned wa0 = L_WQTOP - 51200 + trl, wt0 = L_WQTOP - 51200 + trr + (lane & 3) * 40 + 32, wa1 = wa0 - 51200, wt1 = wt0 - 51200;
      unsigned wov = lane < 50 ? (bi * 200 + lw * 50 + lane) * 128 : OOB, ca = cnt_addr(L_CNT, lane), one = 1;
      v4f eps4 = {1e-6f, 1e-6f, 1e-6f, 1e-6f};
      PINV(wa0); PINV(wt0); PINV(wa1); PINV(wt1); PINV(wov); PINV(ca); PINV(one); PINV(eps4);
#define ARRIVE(off) asm volatile("ds_add_u32 %0, %1 offset:%2" :: "v"(ca), "v"(one), "n"(off) : "memory")
      wq_block(8 + lw, 0, 5); wq_block(12 + lw, 0, 5); wq_block(16 + lw, 0, 5);
      waitv<0>();
      ARRIVE(12);
      warm(ro, wov, 0);
      v4f acc[2][5];
      __builtin_amdgcn_s_setprio(3);
      lds_wait<1>(L_CNT + 12, 4);
      v4u aC, aB, aZ, aX;
      v4u wf[2][5];
#define QRD(j) do { const unsigned o_ = (9 - (j) % 10) * 5120; _Pragma("unroll") for (int n = 0; n < 5; ++n) { \
        const unsigned a_ = n < 4 ? ((j) < 10 ? wa0 : wa1) + n * 40 + o_ : ((j) < 10 ? wt0 : wt1) + o_; wf[(j) & 1][n] = cat(ldtr(a_), ldtr(a_ + 2560)); } } while (0)
      QRD(0);
      lds_wait<1>(L_CNT + 32 + lw * 4, 2);
      __builtin_amdgcn_s_setprio(0);
      SCHED();
#pragma unroll
      for (int j = 0; j < 20; ++j) {
        if (j < 19) QRD(j + 1);
        if (j == 19) { aC = lds128(L_TAB + lane * 16); aB = lds128(L_TAB + 1024 + lane * 16); aZ = lds128(L_TAB + 2048 + lane * 16); aX = lds128(L_TAB + 3072 + lane * 16); }
#pragma unroll
        for (int n = 0; n < 5; ++n)
#pragma unroll
          for (int m = 0; m < 2; ++m) acc[m][n] = mfma(wf[j & 1][n], xr[j][m], j ? acc[m][n] : eps4);
#pragma unroll
        for (int i = 0; i < 10; ++i) { __builtin_amdgcn_sched_group_barrier(0x8, 1, 0); __builtin_amdgcn_sched_group_barrier(0x100, 1, 0); }
        SCHED();
        if (j == 3) ARRIVE(28); if (j == 8) ARRIVE(60); if (j == 13) ARRIVE(48);
      }
#undef QRD
#undef ARRIVE
      v2u pq[2][5];
#pragma unroll
      for (int m = 0; m < 2; ++m)
#pragma unroll
        for (int n = 0; n < 5; ++n) pq[m][n] = phi4(acc[m][n]);
      v4u sq[2][4], sp[2][2];
#pragma unroll
      for (int m = 0; m < 2; ++m)
#pragma unroll
        for (int h = 0; h < 4; ++h) { sq[m][h] = cat(pq[m][h], pq[m][4]); PINV(sq[m][h]); if (h < 2) { sp[m][h] = cat(pq[m][2 * h], pq[m][2 * h + 1]); PINV(sp[m][h]); } }
      const unsigned ob = WS_OF + ((rt * 8 + 2 * lw) * 8 + g) * 2560;
      unsigned aA[4], oa = ob + lane * 16, oa1 = oa + 20480, ot = ob + aX.z; PINV(oa); PINV(oa1); PINV(ot);
#pragma unroll
      for (int h = 0; h < 4; ++h) { aA[h] = L_KVC + h * 896 + lane * 8; PINV(aA[h]); }
      waitv<0>();
      __builtin_amdgcn_s_setprio(3);
      lds_wait<0>(L_CNT + 8, 4);
      {
        const v2u z2 = {0, 0};
        v4u fo[4];
        const v4u fn0 = cat(lds64(aZ[0]), lds64(aZ[1])), fn1 = cat(lds64(aZ[2]), lds64(aZ[3])), fn2 = cat(z2, lds64(aX.y));
#pragma unroll
        for (int h = 0; h < 4; ++h) fo[h] = cat(lds64(aA[h]), lds64(aC[h]));
        const v4u ft0 = cat(lds64(aB[0]), lds64(aB[1])), ft1 = cat(lds64(aB[2]), lds64(aB[3])), ft2 = cat(z2, lds64(aX.x));
        SCHED();
        const v4f zero = {0, 0, 0, 0}, eps = {1e-6f, 1e-6f, 1e-6f, 1e-6f};
        v4f den[2], no[2][4], nt[2], rc[2];
        den[0] = mfma(fn0, sp[0][0], eps); den[1] = mfma(fn0, sp[1][0], eps); SCHED();
        den[0] = mfma(fn1, sp[0][1], den[0]); den[1] = mfma(fn1, sp[1][1], den[1]); SCHED();
        den[0] = mfma(fn2, sq[0][0], den[0]); den[1] = mfma(fn2, sq[1][0], den[1]); SCHED();
        no[0][0] = mfma(fo[0], sq[0][0], zero); no[0][1] = mfma(fo[1], sq[0][1], zero); SCHED();
#pragma unroll
        for (int i = 0; i < 6; ++i) {
          const int m = i >= 2, h = (i + 2) & 3;
          no[m][h] = mfma(fo[h], sq[m][h], zero);
          if (i < 4) { rc[i >> 1][2 * (i & 1)] = __builtin_amdgcn_rcpf(den[i >> 1][2 * (i & 1)]); rc[i >> 1][2 * (i & 1) + 1] = __builtin_amdgcn_rcpf(den[i >> 1][2 * (i & 1) + 1]); }
          SCHED();
        }
#pragma unroll
        for (int i = 0; i < 4; ++i) {
          const int m = i >> 1, h = 2 * (i & 1);
          if (i < 3) { nt[0] = mfma(i == 0 ? ft0 : i == 1 ? ft1 : ft2, i < 2 ? sp[0][i & 1] : sq[0][0], i ? nt[0] : zero); nt[1] = mfma(i == 0 ? ft0 : i == 1 ? ft1 : ft2, i < 2 ? sp[1][i & 1] : sq[1][0], i ? nt[1] : zero); }
          ST128(cat(pk4(no[m][h] * rc[m][h]), pk4(no[m][h + 1] * rc[m][h + 1])), bws, m ? oa1 : oa, (i & 1) * 1024, 16);
          SCHED();
        }
#pragma unroll
        for (int m = 0; m < 2; ++m) { const float ra = (lane & 16) ? rc[m][1] : rc[m][0], rb = (lane & 16) ? rc[m][3] : rc[m][2]; nt[m] *= (lane & 32) ? rb : ra; }
        ST128(conv_pack(nt[0], nt[1]), bws, ot, 0, 16);
        lds_arrive(L_CNT + 24, lane);
        waitv<0>();
        set_flag(p.ws, FL_O + (rt * 4 + lw) * 8 + g, tag, 1, lane);
        lds_arrive(L_CNT + 16, lane);
      }
      __builtin_amdgcn_s_setprio(0);
      out_offsets();
      lds_wait<1>(L_CNT + 24, 8);
      gate(L_QTB, L_YTB, 1);
    }
  } else {
    if (wv < 4) {
      GET_TAG();
      out_offsets();
      const ConvArgs ca = conv_args(L_CONV + wv * L_CH, dbits, WS_YT + c0 * 4096, lane);
      waitv<0>(); bar();
      {
        const int tg = lane & 7;
        unsigned ka = L_KIMG + tg * 2576 + (8 * wv + (lane >> 3)) * 4, sa = L_SW + (8 * wv + (lane >> 3)) * 16;
        unsigned uo = WS_UT + (160 * cq + 16 * wv + 2 * (lane >> 3)) * 4096 + 2 * (2040 - t0 - 8 * tg);
        unsigned kr[11], vr[8]; v4u sw;
#define PRD() do { PINV(ka); sw = lds128(sa); _Pragma("unroll") for (int j = 0; j < 11; ++j) kr[j] = lds32(ka + (j < 3 ? (5 + j) * 320 : 2576 + (j - 3) * 320)); \
          _Pragma("unroll") for (int j = 0; j < 8; ++j) vr[j] = lds32(ka + (L_VIMG - L_KIMG) + j * 320); ka += 128; sa += 512; } while (0)
        PRD();
#pragma unroll
        for (int it = 0; it < 3; ++it) {
          if (it == 2 && wv >= 2) break;
          unsigned pa[10], pb[10]; v2f vv[8];
#pragma unroll
          for (int j = 0; j < 10; ++j) { pa[j] = __builtin_amdgcn_perm(kr[j], kr[j + 1], 0x05040100u); pb[j] = __builtin_amdgcn_perm(kr[j], kr[j + 1], 0x07060302u); }
#pragma unroll
          for (int j = 0; j < 8; ++j) vv[j] = (v2f){blo(vr[j]), bhi(vr[j])};
          const v4u w = sw;
          SCHED();
          if (it < 2) PRD();
          SCHED();
          v2f uu[8];
#pragma unroll
          for (int j = 0; j < 8; ++j) uu[j] = (v2f){dot0(pa[j], w.y), dot0(pb[j], w.w)};
          SCHED();
#pragma unroll
          for (int j = 0; j < 8; ++j) uu[j] = (v2f){dot2(pa[j + 2], w.x, uu[j].x), dot2(pb[j + 2], w.z, uu[j].y)};
          SCHED();
#pragma unroll
          for (int j = 0; j < 8; ++j) uu[j] *= vv[j];
          SCHED();
          v4u ua = {pk2(uu[7].x, uu[6].x), pk2(uu[5].x, uu[4].x), pk2(uu[3].x, uu[2].x), pk2(uu[1].x, uu[0].x)};
          v4u ub = {pk2(uu[7].y, uu[6].y), pk2(uu[5].y, uu[4].y), pk2(uu[3].y, uu[2].y), pk2(uu[1].y, uu[0].y)};
          PINV(ua); PINV(ub);
          SCHED();
          ST128(ua, bws, uo, it * 64 * 4096, 16); ST128(ub, bws, uo, (it * 64 + 1) * 4096, 16);
          SCHED();
        }
        lds_arrive(L_CNT + 40, lane);
#undef PRD
      }
      arrive_flag(L_CNT + 4, 4, p.ws, FL_U + cq * 32 + tt, tag, lane);
      lds_wait<1>(L_CNT + 4, 4);
      __builtin_amdgcn_s_sleep(UDELAY);
      wait_flags(p.ws, rep + FL_U + (g >> 1) * 32 + wv * 8, 8, tag, lane);
#pragma unroll
      for (int c = 4; c >= 0; --c) dma_sc1(rws, lane * 16, WS_UT + (c5 + c) * 4096 + (3 - wv) * 1024, L_CONV + c * L_CH + L_UOFF + (3 - wv) * 1024);
      waitv<4>();
      lds_arrive(L_CNT + 56, lane); lds_wait<0>(L_CNT + 56, 8);
      conv_wave((4 - wv) * (int)L_CH + 128 * wv, ca, p.ws, wv, lane);
      arrive_flag(L_CNT + 8, 4, p.ws, FL_Y + g * 16 + rt, tag, lane);
      lds_wait<1>(L_CNT + 20, 4);
    } else {
      if (lw == 2) warm_x(2); else warm_w(2 + lw - (lw >> 1));
      GET_TAG();
      unsigned w1o = (lw & 1) ? off_w(5 + (lw >> 1)) : off_x(3 + (lw >> 1)), wqo;
      { const unsigned r5 = lane / 5, rho = 12 * lw + r5; int kb = k0 + (rho >> 2); kb = kb >= 10 ? kb - 10 : kb;
        wqo = (lane < 60 && rho < 40) ? (kb * 64 + iq * 4 + (rho & 3)) * 3840 + (xcd & 1) * 640 + (lane - r5 * 5) * 128 : OOB; PINV(wqo); }
      waitv<1>(); bar();
      warm((lw & 1) ? rw : rx, w1o, 0);
      const v4i rh = mk_rsrc(p.h, 640 * 4096);
#pragma unroll
      for (int c = 0; c < 5; ++c) dma(rh, lane * 16, (c5 + c) * 4096 + lw * 1024, L_CONV + c * L_CH + 32 + 3 * L_CS + lw * 1024);
#pragma unroll 1
      for (int c = lw; c < 5; c += 4) { const unsigned cb = L_CONV + c * L_CH;
        if (lane < 38) sts128(lane < 30 ? cb + L_UOFF + 4096 + lane * 16 : cb + ((lane - 30) >> 1) * L_CS + (lane & 1) * 16, (v4u){0, 0, 0, 0}); }
      waitv<0>();
      lds_arrive(L_CNT + 32, lane);
      lds_wait<2>(L_CNT + 40, 4);
      warm(rw, wqo, 0);
      row_offsets(); out_offsets();
      lds_wait<1>(L_CNT + 32, 4);
#pragma unroll 1
      for (int c = 4; c >= 0; --c) {
        const unsigned a = L_CONV + c * L_CH + 32 + lw * 1024 + lane * 16;
        const v4u d = lds128(a + 3 * L_CS); const v2u m = lds64(a + 3 * L_CS - 8);
        const unsigned e0 = __builtin_amdgcn_alignbit(m.y, m.x, 16), e1 = __builtin_amdgcn_alignbit(d.x, m.y, 16), e2 = __builtin_amdgcn_alignbit(d.y, d.x, 16),
                       e3 = __builtin_amdgcn_alignbit(d.z, d.y, 16), e4 = __builtin_amdgcn_alignbit(d.w, d.z, 16);
        sts128(a, (v4u){e0, e1, e2, e3}); sts128(a + L_CS, (v4u){m.y, d.x, d.y, d.z}); sts128(a + 2 * L_CS, (v4u){e1, e2, e3, e4});
        if (c == 4) lds_arrive(L_CNT + 56, lane);
      }
      lds_arrive(L_CNT + 60, lane);
      unsigned yo5 = WS_YT + cd * 4096 + 16 * l15 + 256 * (grp >> 1), wov = lane < 50 ? (bi * 200 + lw * 50 + lane) * 128 : OOB; PINV(yo5); PINV(wov);
      lds_wait<2>(L_CNT + 56, 8);
      __builtin_amdgcn_s_setprio(3);
      warm(ro, wov, 0);
      waitv<0>();
      v4u wr[25];
      {
        const __amdgpu_buffer_rsrc_t bo = __builtin_amdgcn_make_buffer_rsrc((void*)p.wo, 0, 640 * 1280, 0x00020000);
#pragma unroll
        for (int j = 0; j < 25; ++j) wr[j] = __builtin_amdgcn_raw_buffer_load_b128(bo, vo5[j % 5], ((lw ^ rot) * 5 + j / 5) * 32 * 1280 + gct * 160, 0);
      }
      SCHED();
      v4f ya, yb;
      {
        const float dv = blo(dbits);
        const unsigned ub = L_CONV5 + L_UOFF + 4088 - 32 * l15 - 8 * grp;
        const v2u ua = lds64(ub - 512 * lw), uc = lds64(ub - 512 * (7 - lw));
        ya = (v4f){mul1(dv, bhi(ua.y)), mul1(dv, blo(ua.y)), mul1(dv, bhi(ua.x)), mul1(dv, blo(ua.x))};
        yb = (v4f){mul1(dv, bhi(uc.y)), mul1(dv, blo(uc.y)), mul1(dv, bhi(uc.x)), mul1(dv, blo(uc.x))};
        PINV(ya); PINV(yb);
      }
      lds_wait<2>(L_CNT + 12, 4);
#pragma unroll
      for (int w = 0; w < 4; ++w) { ya = add4(ya, __builtin_bit_cast(v4f, lds128(L_RED + (lw * 4 + w) * 1024 + lane * 16))); yb = add4(yb, __builtin_bit_cast(v4f, lds128(L_RED + ((7 - lw) * 4 + w) * 1024 + lane * 16))); }
      conv_store(conv_pack(ya, yb), lw, yo5, p.ws, lane);
      lds_arrive(L_CNT + 12, lane); lds_wait<1>(L_CNT + 12, 8);
      {
        const v4i rq = mk_rsrc(p.q, 2048 * 1280);
        if (lw < 2) {
#pragma unroll
          for (int j = 0; j < 5; ++j) dma(rq, vo5[j], (t0 + lw * 32) * 1280 + cq * 320, L_QT + lw * 5120 + j * 1024);
        } else warm(rq, (t0 + (lw - 2) * 32 + (lane >> 1)) * 1280 + cq * 320 + 160 + (lane & 1) * 156, 0);
      }
      waitv<0>();
      bar();
      __builtin_amdgcn_s_setprio(0);
      unsigned wb = L_CONV + lw * 25600 + lane * 16; PINV(wb);
#pragma unroll
      for (int j = 0; j < 25; ++j) sts128(wb + j * 1024, wr[j]);
      lds_arrive(L_CNT + 20, lane);
      gate(L_QT, L_YT, 0);
      if (b == 128) {
        __builtin_amdgcn_s_sleep(12);
        wait_flags(p.ws, FL_U + lw * 64, 64, tag, lane);
        lds_arrive(L_CNT + 68, lane);
        if (lw == 0) { lds_wait<1>(L_CNT + 68, 4); if (lane == 0) __hip_atomic_store((GAS u64*)p.ws, tag, RLX_AGENT); }
      }
    }
  }
  if (wv < 4) {
    const int mt = (gr0 >> 4) + wv;
    unsigned wbq[4], wbt[4];
#pragma unroll
    for (int y = 0; y < 4; ++y) { const unsigned base = isA ? L_CONV + y * 25600 : y ? L_WQTOP - y * 25600 : 0;
      wbq[y] = base + trl; wbt[y] = base + (40 * grp + (l15 >> 2) + 16) * 160 + 8 * (lane & 3); PINV(wbq[y]); PINV(wbt[y]); }
    unsigned otl = (grp >> 1) * 2560 + 2048 + (grp & 1) * 256 + l15 * 16; PINV(otl);
    v4u wf[RING][5];
#define WRD(t, n) do { const int y_ = (t) / 5, s_ = (t) % 5; const unsigned ra = (s_ >> 1) * 80 + (s_ & 1) * 40; \
      wf[(t) % RING][n] = s_ < 4 ? cat(ldtr(wbq[y_] + ra * 160 + (n) * 32), ldtr(wbq[y_] + (ra + 20) * 160 + (n) * 32)) \
                              : cat(ldtr(wbt[y_] + (n) * 32), ldtr(wbt[y_] + 20 * 160 + (n) * 32)); } while (0)
#pragma unroll
    for (int f = 0; f < 5 * RING; ++f) WRD(f / 5, f % 5);
    SCHED();
    if (!isA) { lds_wait<1>(L_CNT + 16, 4); __builtin_amdgcn_s_sleep(ODELAY); }
    else lds_wait<1>(L_CNT + 52, 4);
    wait_flags(p.ws, FL_O + (mt >> 1) * 8, 8, tag, lane);
    v4u of[20];
#define ORD(t) do { const int s_ = (t) % 5; const unsigned oo = WS_OF + (mt * 8 + 2 * (((t) / 5) ^ rot)) * 2560; \
      if (s_ < 4) of[t] = __builtin_amdgcn_raw_buffer_load_b128(bws, lane * 16 + (s_ & 1) * 1024, oo + (s_ >> 1) * 2560, 16); \
      else of[t] = __builtin_amdgcn_raw_buffer_load_b128(bws, otl, oo, 16); } while (0)
#pragma unroll
    for (int t = 0; t < OAHEAD; ++t) { ORD(t); SCHED(); }
    SCHED();
    v4f acc[5];
#pragma unroll
    for (int n = 0; n < 5; ++n) acc[n] = (v4f){0, 0, 0, 0};
#pragma unroll
    for (int t = 0; t < 20; ++t) {
      v4u cur[5];
#pragma unroll
      for (int n = 0; n < 5; ++n) cur[n] = wf[t % RING][n];
#pragma unroll
      for (int f = 0; f < 100 - 5 * RING; ++f) if (t >= 1 && f >= (t - 1) * PACE / 4 && f < t * PACE / 4) WRD(RING + f / 5, f % 5);
#pragma unroll
      for (int n = 0; n < 5; ++n) acc[n] = mfma(cur[n], of[t], acc[n]);
      if (t + OAHEAD < 20) ORD(t + OAHEAD);
      __builtin_amdgcn_sched_group_barrier(0x8, 1, 0); __builtin_amdgcn_sched_group_barrier(0x100, 2, 0);
      __builtin_amdgcn_sched_group_barrier(0x8, 1, 0); __builtin_amdgcn_sched_group_barrier(0x100, 1, 0); __builtin_amdgcn_sched_group_barrier(0x20, 1, 0);
      __builtin_amdgcn_sched_group_barrier(0x8, 1, 0); __builtin_amdgcn_sched_group_barrier(0x100, 2, 0);
      __builtin_amdgcn_sched_group_barrier(0x8, 1, 0); __builtin_amdgcn_sched_group_barrier(0x100, 1, 0); __builtin_amdgcn_sched_group_barrier(0x20, 1, 0);
      __builtin_amdgcn_sched_group_barrier(0x8, 1, 0); __builtin_amdgcn_sched_group_barrier(0x100, 2, 0);
      SCHED();
    }
#undef WRD
#undef ORD
    v2u res[5];
#pragma unroll
    for (int n = 0; n < 5; ++n) res[n] = pk4(acc[n]);
    const unsigned stb = (isA ? L_STA : L_STB) + wv * 2560;
    OUT_TILE(res, stb, gr0 * 2560 + 1280 + gct * 160, 0);
    asm volatile(".Lfk_tail:" ::: "memory");
  }
}

extern "C" void kernel_launch(void* const* d_in, const int* in_sizes, int n_in, void* d_out, int out_size, void* d_ws, size_t ws_size, hipStream_t stream) {
  Params p;
  p.q = (const u16*)d_in[0]; p.k = (const u16*)d_in[1]; p.v = (const u16*)d_in[2]; p.sk = (const u16*)d_in[3]; p.h = (const u16*)d_in[4];
  p.Dv = (const u16*)d_in[5]; p.x = (const u16*)d_in[6]; p.w = (const u16*)d_in[7]; p.wo = (const u16*)d_in[8];
  p.out = (u16*)d_out; p.ws = (char*)d_ws;
  fused_kernel<<<256, 512, 0, stream>>>(p);
}
```
